```python
import torch

def _softmax(x, axis=-1):
    return torch.softmax(x, dim=axis)
def _gelu(x):
    return torch.nn.functional.gelu(x, approximate="tanh")
def _silu(x):
    return torch.nn.functional.silu(x)
def _sigmoid(x):
    return torch.sigmoid(x)
def _layernorm(x, axis=-1, eps=1e-5):
    m = x.mean(dim=axis, keepdim=True)
    v = x.var(dim=axis, keepdim=True, unbiased=False)
    return (x - m) / torch.sqrt(v + eps)
def _scatter_add(tgt, idx, src, axis=0):
    return tgt.clone().index_add_(axis, idx, src)
def _segment_sum(src, seg, n):
    out = torch.zeros((n,) + src.shape[1:], dtype=src.dtype)
    return out.index_add_(0, seg, src)
def _pad(x, pw):
    flat = tuple(v for pair in reversed(pw) for v in pair)
    return torch.nn.functional.pad(x, flat)

kernel_name = 'sequential_preLN__mha_block_cached__glu_gated_geglu__decode'

def setup_inputs():

    _w = (lambda a: (lambda t: t.to(torch.bfloat16) if t.is_floating_point() else t)(torch.as_tensor(a)))
    def _A_setup():
        B, Sq, D = 2, 1, 576
        H, Sk, Dh = 32, 8192, 128
        return {
            "x": _w((lambda *s: torch.randn(*s, dtype=torch.bfloat16) * 0.05)(B, Sq, D)),
            "k_cache": _w((lambda *s: torch.randn(*s, dtype=torch.bfloat16) * 1.6)(B, H, Sk, Dh)),
            "v_cache": _w((lambda *s: torch.randn(*s, dtype=torch.bfloat16) * 0.8)(B, H, Sk, Dh)),
            "wq": _w((lambda *s: torch.randn(*s, dtype=torch.bfloat16) * 1.6)(D, H * Dh) * (D ** -0.5)),
            "wo": _w((lambda *s: torch.randn(*s, dtype=torch.bfloat16) * 0.8)(H * Dh, D) * ((H * Dh) ** -0.5)),
        }
    _A_in = _A_setup()

    def _B_setup():
        B, S, Dm, Df = 2, 1, 576, 4608
        return {
            "x": _w((lambda *s: torch.randn(*s, dtype=torch.bfloat16) * 0.05)(B, S, Dm)),
            "w_gate": _w((lambda *s: torch.randn(*s, dtype=torch.bfloat16) * 0.8)(Dm, Df) * (Dm ** -0.5)),
            "w_up": _w((lambda *s: torch.randn(*s, dtype=torch.bfloat16) * 0.8)(Dm, Df) * (Dm ** -0.5)),
            "w_down": _w((lambda *s: torch.randn(*s, dtype=torch.bfloat16) * 0.8)(Df, Dm) * (Df ** -0.5)),
        }
    _B_in = _B_setup()

    return {
        "A_x": _A_in["x"],
        "A_k_cache": _A_in["k_cache"],
        "A_v_cache": _A_in["v_cache"],
        "A_wq": _A_in["wq"],
        "A_wo": _A_in["wo"],
        "B_x": _B_in["x"],
        "B_w_gate": _B_in["w_gate"],
        "B_w_up": _B_in["w_up"],
        "B_w_down": _B_in["w_down"],
    }

def reference(A_x, A_k_cache, A_v_cache, A_wq, A_wo, B_x, B_w_gate, B_w_up, B_w_down):
    def _A_ref(x, k_cache, v_cache, wq, wo):
        B, Sq, D = x.shape
        H, Dh = 32, 128
        q = (lambda x, *d: x.permute(*d))((x @ wq).reshape(B, Sq, H, Dh), 0, 2, 1, 3)
        scores = torch.einsum("bhqd,bhkd->bhqk", q, k_cache) * (Dh ** -0.5)
        p = _softmax(scores, axis=-1)
        o = torch.einsum("bhqk,bhkd->bhqd", p, v_cache)
        return (lambda x, *d: x.permute(*d))(o, 0, 2, 1, 3).reshape(B, Sq, H * Dh) @ wo

    def _B_ref(x, w_gate, w_up, w_down):
        g = torch.einsum("bsd,df->bsf", x, w_gate)
        u = torch.einsum("bsd,df->bsf", x, w_up)
        h = _gelu(g) * u
        return torch.einsum("bsf,fd->bsd", h, w_down)

    x = A_x
    h = x + _A_ref(_layernorm(x, axis=-1), A_k_cache, A_v_cache, A_wq, A_wo)
    h2 = h + _B_ref(_layernorm(h, axis=-1), B_w_gate, B_w_up, B_w_down)
    return h2
```

```cpp
#include <hip/hip_runtime.h>
#include <hip/hip_bf16.h>
#include <stdint.h>

#define MIMIC 0

typedef unsigned u32x4 __attribute__((ext_vector_type(4)));
typedef float f32x4 __attribute__((ext_vector_type(4)));
typedef __bf16 bf16x2 __attribute__((ext_vector_type(2)));

constexpr int B = 2, D = 576, H = 32, SK = 8192, DH = 128, DF = 4608;
constexpr int NSPLIT = 8;

__device__ __forceinline__ float bf2f(unsigned short b) { return __uint_as_float(((unsigned)b) << 16); }
__device__ __forceinline__ float rbf(float f) {
  unsigned r; asm volatile("v_cvt_pk_bf16_f32 %0, %1, %1" : "=v"(r) : "v"(f));
  return __uint_as_float(r << 16);
}
__device__ __forceinline__ float rb(float f) { return MIMIC ? rbf(f) : f; }
__device__ __forceinline__ float dot2(unsigned k, unsigned q, float acc) {
  return __builtin_amdgcn_fdot2_f32_bf16(__builtin_bit_cast(bf16x2, k), __builtin_bit_cast(bf16x2, q), acc, false);
}
template <int CTRL> __device__ __forceinline__ float dpp(float x) {
  return __builtin_bit_cast(float, __builtin_amdgcn_mov_dpp(__builtin_bit_cast(int, x), CTRL, 0xf, 0xf, true));
}
constexpr int XOR1 = 0xB1, XOR2 = 0x4E, XOR7 = 0x141, XOR8 = 0x128;

__device__ __forceinline__ float wave_sum(float v) {
  for (int m = 32; m >= 1; m >>= 1) v += __shfl_xor(v, m);
  return v;
}

__device__ void ln_row(const float* xs, float* xn, float* red, int tid, int nthr) {
  float s = 0.f;
  for (int i = tid; i < D; i += nthr) s += xs[i];
  s = wave_sum(s);
  if ((tid & 63) == 0) red[tid >> 6] = s;
  __syncthreads();
  float tot = 0.f;
  for (int w = 0; w < nthr / 64; ++w) tot += red[w];
  const float mean = tot * (1.0f / D);
  __syncthreads();
  float v = 0.f;
  for (int i = tid; i < D; i += nthr) { float d = xs[i] - mean; v += d * d; }
  v = wave_sum(v);
  if ((tid & 63) == 0) red[tid >> 6] = v;
  __syncthreads();
  float tv = 0.f;
  for (int w = 0; w < nthr / 64; ++w) tv += red[w];
  const float var = rb(tv * (1.0f / D));
  const float mb = rb(mean);
  const float den = rb(sqrtf(rb(var + 1e-5f)));
  for (int i = tid; i < D; i += nthr) xn[i] = rb(rb(xs[i] - mb) / den);
  __syncthreads();
}

__global__ void __launch_bounds__(256) k_q(const unsigned short* x, const unsigned short* wq, float* q) {
  __shared__ float xs[D], xn[B][D], red[4], part[4][B][64];
  const int tid = threadIdx.x;
  for (int b = 0; b < B; ++b) {
    for (int i = tid; i < D; i += 256) xs[i] = bf2f(x[b * D + i]);
    __syncthreads();
    ln_row(xs, xn[b], red, tid, 256);
  }
  const int col = blockIdx.x * 64 + (tid & 63), w = tid >> 6;
  float a0 = 0.f, a1 = 0.f;
  for (int k = w; k < D; k += 4) { float wv = bf2f(wq[(size_t)k * (H * DH) + col]); a0 += xn[0][k] * wv; a1 += xn[1][k] * wv; }
  part[w][0][tid & 63] = a0; part[w][1][tid & 63] = a1;
  __syncthreads();
  if (tid < 128) { int b = tid >> 6, c = tid & 63; float s = part[0][b][c] + part[1][b][c] + part[2][b][c] + part[3][b][c];
    q[b * H * DH + blockIdx.x * 64 + c] = rbf(s); }
}

__global__ void __launch_bounds__(256) k_attn(const float* q, const unsigned short* kc, const unsigned short* vc, float* pml, float* po) {
  __shared__ float mrg[4][4 + DH];
  const int s = blockIdx.x, h = blockIdx.y, b = blockIdx.z;
  const int tid = threadIdx.x, lane = tid & 63, wave = __builtin_amdgcn_readfirstlane(tid >> 6), r = lane >> 4, c = lane & 15;
  constexpr int KEYS = SK / NSPLIT;
  constexpr int TPI = 8;
  constexpr int ITERS = KEYS / (4 * 4 * TPI);
  unsigned qp[4];
  { const float* qq = q + (b * H + h) * DH + 8 * c;
    for (int i = 0; i < 4; ++i) qp[i] = (__float_as_uint(qq[2 * i]) >> 16) | (__float_as_uint(qq[2 * i + 1]) & 0xffff0000u); }
  const size_t base = ((size_t)(b * H + h) * SK + (size_t)s * KEYS + wave * (4 * TPI) + r) * DH + 8 * c;
  const u32x4* kp = (const u32x4*)(kc + base);
  const u32x4* vp = (const u32x4*)(vc + base);
  constexpr int TS = 4 * DH / 8;
  constexpr int IS = 4 * 4 * TPI * DH / 8;
  u32x4 Kn[TPI], Vn[TPI];
#pragma unroll
  for (int t = 0; t < TPI; ++t) { Kn[t] = __builtin_nontemporal_load(kp + t * TS); }
#pragma unroll
  for (int t = 0; t < TPI; ++t) { Vn[t] = __builtin_nontemporal_load(vp + t * TS); }
  float m = -INFINITY, l = 0.f, acc[8];
#pragma unroll
  for (int k = 0; k < 8; ++k) acc[k] = 0.f;
  constexpr float SCALE = 0.088388347648318440f, LOG2E = 1.4426950408889634f;
  for (int it = 0; it < ITERS; ++it) {
    u32x4 Kc[TPI], Vc[TPI];
#pragma unroll
    for (int t = 0; t < TPI; ++t) { Kc[t] = Kn[t]; Vc[t] = Vn[t]; }
    if (it + 1 < ITERS) {
#pragma unroll
      for (int t = 0; t < TPI; ++t) { Kn[t] = __builtin_nontemporal_load(kp + (it + 1) * IS + t * TS); }
#pragma unroll
      for (int t = 0; t < TPI; ++t) { Vn[t] = __builtin_nontemporal_load(vp + (it + 1) * IS + t * TS); }
    }
    float sc[TPI];
    float pm = -INFINITY;
#pragma unroll
    for (int t = 0; t < TPI; ++t) {
      float d = 0.f;
#pragma unroll
      for (int k = 0; k < 4; ++k) d = dot2(Kc[t][k], qp[k], d);
      d += dpp<XOR8>(d); d += dpp<XOR7>(d); d += dpp<XOR2>(d); d += dpp<XOR1>(d);
      d = rb(rb(d) * SCALE) * LOG2E;
      sc[t] = d; pm = fmaxf(pm, d);
    }
    const float mn = fmaxf(m, pm);
    const float alpha = __builtin_amdgcn_exp2f(m - mn);
    m = mn; l *= alpha;
#pragma unroll
    for (int k = 0; k < 8; ++k) acc[k] *= alpha;
#pragma unroll
    for (int t = 0; t < TPI; ++t) {
      const float p = __builtin_amdgcn_exp2f(sc[t] - mn);
      l += p;
#pragma unroll
      for (int k = 0; k < 4; ++k) {
        acc[2 * k] = fmaf(p, __uint_as_float(Vc[t][k] << 16), acc[2 * k]);
        acc[2 * k + 1] = fmaf(p, __uint_as_float(Vc[t][k] & 0xffff0000u), acc[2 * k + 1]);
      }
    }
  }
  float M = fmaxf(m, __shfl_xor(m, 16)); M = fmaxf(M, __shfl_xor(M, 32));
  const float wgt = __builtin_amdgcn_exp2f(m - M);
  l *= wgt; l += __shfl_xor(l, 16); l += __shfl_xor(l, 32);
#pragma unroll
  for (int k = 0; k < 8; ++k) { float a = acc[k] * wgt; a += __shfl_xor(a, 16); a += __shfl_xor(a, 32); acc[k] = a; }
  if (r == 0) {
#pragma unroll
    for (int k = 0; k < 8; ++k) mrg[wave][4 + 8 * c + k] = acc[k];
    if (c == 0) { mrg[wave][0] = M; mrg[wave][1] = l; }
  }
  __syncthreads();
  if (tid < DH) {
    float MM = fmaxf(fmaxf(mrg[0][0], mrg[1][0]), fmaxf(mrg[2][0], mrg[3][0]));
    float L = 0.f, o = 0.f;
    for (int w = 0; w < 4; ++w) { float wt = __builtin_amdgcn_exp2f(mrg[w][0] - MM); L += wt * mrg[w][1]; o += wt * mrg[w][4 + tid]; }
    const int slot = (b * H + h) * NSPLIT + s;
    po[slot * DH + tid] = o;
    if (tid == 0) { pml[slot * 2] = MM; pml[slot * 2 + 1] = L; }
  }
}

__global__ void __launch_bounds__(288) k_wo(const float* pml, const float* po, const unsigned short* wo, float* ypart) {
  __shared__ float os[B][32];
  __shared__ float red[4][B][D];
  const int h = blockIdx.x >> 2, j = blockIdx.x & 3, tid = threadIdx.x;
  if (tid < 64) {
    const int b = tid >> 5, d = j * 32 + (tid & 31);
    const int slot0 = (b * H + h) * NSPLIT;
    float M = -INFINITY;
    for (int s = 0; s < NSPLIT; ++s) M = fmaxf(M, pml[(slot0 + s) * 2]);
    float L = 0.f, o = 0.f;
    for (int s = 0; s < NSPLIT; ++s) { float wt = __builtin_amdgcn_exp2f(pml[(slot0 + s) * 2] - M); L += wt * pml[(slot0 + s) * 2 + 1]; o += wt * po[(slot0 + s) * DH + d]; }
    os[b][tid & 31] = rb(o / L);
  }
  __syncthreads();
  const int cg = tid % 72, rg = tid / 72;
  float a[B][8];
  for (int k = 0; k < 8; ++k) { a[0][k] = 0.f; a[1][k] = 0.f; }
#pragma unroll
  for (int rr = 0; rr < 8; ++rr) {
    const int row = rg * 8 + rr;
    u32x4 w = *(const u32x4*)(wo + (size_t)(h * DH + j * 32 + row) * D + cg * 8);
    for (int k = 0; k < 4; ++k) {
      float w0 = __uint_as_float(w[k] << 16), w1 = __uint_as_float(w[k] & 0xffff0000u);
      a[0][2 * k] += os[0][row] * w0; a[0][2 * k + 1] += os[0][row] * w1;
      a[1][2 * k] += os[1][row] * w0; a[1][2 * k + 1] += os[1][row] * w1;
    }
  }
  for (int k = 0; k < 8; ++k) { red[rg][0][cg * 8 + k] = a[0][k]; red[rg][1][cg * 8 + k] = a[1][k]; }
  __syncthreads();
  for (int i = tid; i < B * D; i += 288) { int b = i / D, d = i % D; ypart[(size_t)blockIdx.x * B * D + i] = red[0][b][d] + red[1][b][d] + red[2][b][d] + red[3][b][d]; }
}

__global__ void __launch_bounds__(256) k_hred(const float* ypart, int nparts, const unsigned short* x, const float* hin, float* hout, unsigned short* out) {
  __shared__ float red[4][64];
  const int tid = threadIdx.x, c = tid & 63, w = tid >> 6, i = blockIdx.x * 64 + c;
  float s = 0.f;
  for (int p = w; p < nparts; p += 4) s += ypart[(size_t)p * B * D + i];
  red[w][c] = s;
  __syncthreads();
  if (tid < 64) {
    float t = rb(red[0][c] + red[1][c] + red[2][c] + red[3][c]);
    float base = x ? bf2f(x[i]) : hin[i];
    float r = base + t;
    if (hout) hout[i] = rb(r);
    if (out) out[i] = (unsigned short)(__float_as_uint(rbf(r)) >> 16);
  }
}

__global__ void __launch_bounds__(288) k_ffn(const float* hbuf, const unsigned short* wg, const unsigned short* wu, const unsigned short* wd, float* ypart) {
  __shared__ float xs[D], xn[B][D], red5[8], act[B][32];
  __shared__ float part[2][B][64][33];
  __shared__ float red[4][B][D];
  const int tid = threadIdx.x;
  for (int b = 0; b < B; ++b) {
    for (int i = tid; i < D; i += 288) xs[i] = hbuf[b * D + i];
    __syncthreads();
    if (tid < 256) {
    float s = 0.f; for (int i = (tid & 63); i < D; i += 64) s += xs[i];
    s = wave_sum(s); const float mean = s * (1.0f / D);
    float v = 0.f; for (int i = (tid & 63); i < D; i += 64) { float d = xs[i] - mean; v += d * d; }
    v = wave_sum(v);
    const float var = rb(v * (1.0f / D)), mb = rb(mean), den = rb(sqrtf(rb(var + 1e-5f)));
    for (int i = tid; i < D; i += 256) xn[b][i] = rb(rb(xs[i] - mb) / den);
    }
    __syncthreads();
  }
  const int f0 = blockIdx.x * 32;
  if (tid < 256) {
    const int cg = tid & 3, rg = tid >> 2;
    float g[B][8], u[B][8];
    for (int k = 0; k < 8; ++k) { g[0][k] = g[1][k] = u[0][k] = u[1][k] = 0.f; }
#pragma unroll
    for (int rr = 0; rr < 9; ++rr) {
      const int row = rr * 64 + rg;
      u32x4 a = *(const u32x4*)(wg + (size_t)row * DF + f0 + cg * 8);
      u32x4 c = *(const u32x4*)(wu + (size_t)row * DF + f0 + cg * 8);
      const float x0 = xn[0][row], x1 = xn[1][row];
      for (int k = 0; k < 4; ++k) {
        float w0 = __uint_as_float(a[k] << 16), w1 = __uint_as_float(a[k] & 0xffff0000u);
        g[0][2 * k] += x0 * w0; g[0][2 * k + 1] += x0 * w1; g[1][2 * k] += x1 * w0; g[1][2 * k + 1] += x1 * w1;
        w0 = __uint_as_float(c[k] << 16); w1 = __uint_as_float(c[k] & 0xffff0000u);
        u[0][2 * k] += x0 * w0; u[0][2 * k + 1] += x0 * w1; u[1][2 * k] += x1 * w0; u[1][2 * k + 1] += x1 * w1;
      }
    }
    for (int k = 0; k < 8; ++k) { part[0][0][rg][cg * 8 + k] = g[0][k]; part[0][1][rg][cg * 8 + k] = g[1][k]; part[1][0][rg][cg * 8 + k] = u[0][k]; part[1][1][rg][cg * 8 + k] = u[1][k]; }
  }
  __syncthreads();
  if (tid < 64) {
    const int b = tid >> 5, c = tid & 31;
    float g = 0.f, u = 0.f;
    for (int rg = 0; rg < 64; ++rg) { g += part[0][b][rg][c]; u += part[1][b][rg][c]; }
    g = rb(g); u = rb(u);
    const float inner = 0.7978845608028654f * (g + 0.044715f * g * g * g);
    const float ge = rb(0.5f * g * (1.0f + tanhf(inner)));
    act[b][c] = rb(ge * u);
  }
  __syncthreads();
  const int cg = tid % 72, rg = tid / 72;
  float a[B][8];
  for (int k = 0; k < 8; ++k) { a[0][k] = 0.f; a[1][k] = 0.f; }
#pragma unroll
  for (int rr = 0; rr < 8; ++rr) {
    const int row = rg * 8 + rr;
    u32x4 w = *(const u32x4*)(wd + (size_t)(f0 + row) * D + cg * 8);
    for (int k = 0; k < 4; ++k) {
      float w0 = __uint_as_float(w[k] << 16), w1 = __uint_as_float(w[k] & 0xffff0000u);
      a[0][2 * k] += act[0][row] * w0; a[0][2 * k + 1] += act[0][row] * w1;
      a[1][2 * k] += act[1][row] * w0; a[1][2 * k + 1] += act[1][row] * w1;
    }
  }
  for (int k = 0; k < 8; ++k) { red[rg][0][cg * 8 + k] = a[0][k]; red[rg][1][cg * 8 + k] = a[1][k]; }
  __syncthreads();
  for (int i = tid; i < B * D; i += 288) { int b = i / D, d = i % D; ypart[(size_t)blockIdx.x * B * D + i] = red[0][b][d] + red[1][b][d] + red[2][b][d] + red[3][b][d]; }
}

extern "C" void kernel_launch(void* const* d_in, const int* in_sizes, int n_in,
                              void* d_out, int out_size, void* d_ws, size_t ws_size,
                              hipStream_t stream) {
  const unsigned short* x = (const unsigned short*)d_in[0];
  const unsigned short* kc = (const unsigned short*)d_in[1];
  const unsigned short* vc = (const unsigned short*)d_in[2];
  const unsigned short* wq = (const unsigned short*)d_in[3];
  const unsigned short* wo = (const unsigned short*)d_in[4];
  const unsigned short* wg = (const unsigned short*)d_in[6];
  const unsigned short* wu = (const unsigned short*)d_in[7];
  const unsigned short* wd = (const unsigned short*)d_in[8];
  float* ws = (float*)d_ws;
  float* q = ws;
  float* pml = ws + 8192;
  float* po = ws + 16384;
  float* yp = ws + 16384 + 65536;
  float* hb = yp + 144 * 1152 + 1024;
  k_q<<<64, 256, 0, stream>>>(x, wq, q);
  k_attn<<<dim3(NSPLIT, H, B), 256, 0, stream>>>(q, kc, vc, pml, po);
  k_wo<<<128, 288, 0, stream>>>(pml, po, wo, yp);
  k_hred<<<18, 256, 0, stream>>>(yp, 128, x, nullptr, hb, nullptr);
  k_ffn<<<144, 288, 0, stream>>>(hb, wg, wu, wd, yp);
  k_hred<<<18, 256, 0, stream>>>(yp, 144, nullptr, hb, nullptr, (unsigned short*)d_out);
}
```

```cpp
#include <hip/hip_runtime.h>
#include <stdint.h>

#define STAMPS 0
#define TPC 8
#define SKEW 11
#define WO_LESS 4

typedef unsigned short u16;
typedef unsigned u32x4 __attribute__((ext_vector_type(4)));
typedef unsigned u32x2 __attribute__((ext_vector_type(2)));
typedef float f32x4 __attribute__((ext_vector_type(4)));
typedef float f32x2 __attribute__((ext_vector_type(2)));
typedef short s16x4 __attribute__((ext_vector_type(4)));
typedef __bf16 bf16x2 __attribute__((ext_vector_type(2)));
typedef __bf16 bf16x8 __attribute__((ext_vector_type(8)));
#define LAS __attribute__((address_space(3)))
#define GAS __attribute__((address_space(1)))

constexpr int D = 576, H = 32, SK = 8192, DH = 128, DF = 4608;
constexpr int NWG = 256, NTHR = 512;
constexpr int CKEYS = 4 * TPC, CBYTES = CKEYS * 256, CPQ = SK / CKEYS, CMUL = 8 / TPC;
constexpr unsigned kSpin = 1u << 17;

constexpr int WS_EPOCH = 0;
constexpr int WS_HP = 4096;
constexpr int WS_AP = 16384;
constexpr int WS_QP = WS_AP + 256 * 2 * 640;
constexpr int WS_YP = WS_QP + 32 * 8 * 2 * 1024;
constexpr int WS_OP = WS_YP + 72 * 64 * 128;
constexpr int WS_END = WS_OP + 72 * 144 * 128;

constexpr int PITCH = 1056;
constexpr int L_WO = 0, L_WOT = 64 * PITCH;
constexpr int L_G = 0, L_U = 36864, L_D = 73728, L_DT = L_D + 32 * PITCH;
constexpr int L_SM = L_DT + 32 * 128;
constexpr int L_RED = L_SM;
constexpr int L_Q16 = L_RED + 8192;
constexpr int L_O16 = L_Q16 + 256;
constexpr int L_XN = L_O16 + 256;
constexpr int L_GU = L_XN + 2304;
constexpr int L_ACT = L_GU + 1024;
constexpr int L_CTR = L_ACT + 1024;
constexpr int L_END = L_CTR + 16;
static_assert(L_WOT + 64 * 128 <= L_SM, "lds");

__device__ __forceinline__ float lo16(unsigned u) { return __uint_as_float(u << 16); }
__device__ __forceinline__ float hi16(unsigned u) { return __uint_as_float(u & 0xffff0000u); }
__device__ __forceinline__ unsigned cvtpk(float lo, float hi) {
  bf16x2 r; r.x = (__bf16)lo; r.y = (__bf16)hi; return __builtin_bit_cast(unsigned, r);
}
__device__ __forceinline__ float rbf(float f) { return lo16(cvtpk(f, f)); }
__device__ __forceinline__ float dot2(unsigned k, unsigned q, float acc) {
  return __builtin_amdgcn_fdot2_f32_bf16(__builtin_bit_cast(bf16x2, k), __builtin_bit_cast(bf16x2, q), acc, false);
}
template <int CTRL> __device__ __forceinline__ float dpp(float x) {
  return __builtin_bit_cast(float, __builtin_amdgcn_mov_dpp(__builtin_bit_cast(int, x), CTRL, 0xf, 0xf, true));
}
template <int CTRL> __device__ __forceinline__ unsigned dppu(unsigned x) {
  return (unsigned)__builtin_amdgcn_mov_dpp((int)x, CTRL, 0xf, 0xf, true);
}
constexpr int XOR1 = 0xB1, XOR2 = 0x4E, XOR7 = 0x141, XOR8 = 0x128;
__device__ __forceinline__ float x1632_sum(float x) {
  auto s = __builtin_amdgcn_permlane16_swap(__float_as_uint(x), __float_as_uint(x), false, false);
  x = __uint_as_float(s[0]) + __uint_as_float(s[1]);
  auto t = __builtin_amdgcn_permlane32_swap(__float_as_uint(x), __float_as_uint(x), false, false);
  return __uint_as_float(t[0]) + __uint_as_float(t[1]);
}
__device__ __forceinline__ float x1632_max(float x) {
  auto s = __builtin_amdgcn_permlane16_swap(__float_as_uint(x), __float_as_uint(x), false, false);
  x = fmaxf(__uint_as_float(s[0]), __uint_as_float(s[1]));
  auto t = __builtin_amdgcn_permlane32_swap(__float_as_uint(x), __float_as_uint(x), false, false);
  return fmaxf(__uint_as_float(t[0]), __uint_as_float(t[1]));
}
__device__ __forceinline__ float row_sum(float d) { d += dpp<XOR8>(d); d += dpp<XOR7>(d); d += dpp<XOR2>(d); d += dpp<XOR1>(d); return d; }
__device__ __forceinline__ float wave_sum(float d) { return x1632_sum(row_sum(d)); }
__device__ __forceinline__ float fdiv(float a, float b, float rb) {
  const float q = a * rb; return fmaf(fmaf(-q, b, a), rb, q);
}
#define BAR() asm volatile("s_waitcnt lgkmcnt(0)\n\ts_barrier" ::: "memory")
#define VM0() asm volatile("s_waitcnt vmcnt(0)" ::: "memory")
__device__ __forceinline__ void dma16(const void* gsrc, unsigned lds_dst) {
  unsigned keep;
  asm volatile("s_mov_b32 %0, m0\n\ts_mov_b32 m0, %2\n\ts_nop 0\n\tglobal_load_lds_dwordx4 %1, off nt\n\ts_mov_b32 m0, %0" : "=&s"(keep) : "v"(gsrc), "s"(lds_dst) : "memory");
}
typedef __amdgpu_buffer_rsrc_t rsrc_t;
__device__ __forceinline__ u32x4 ld_sc1(rsrc_t r, int off) { return __builtin_amdgcn_raw_buffer_load_b128(r, off, 0, 16); }
__device__ __forceinline__ void st_sc1(rsrc_t r, int off, u32x4 v) { __builtin_amdgcn_raw_buffer_store_b128(v, r, off, 0, 16); }
__device__ __forceinline__ u32x2 trread(LAS unsigned char* p) {
  return __builtin_bit_cast(u32x2, __builtin_amdgcn_ds_read_tr16_b64_v4i16((LAS s16x4*)p));
}
__device__ __forceinline__ f32x4 mfma(u32x4 a, u32x4 b, f32x4 c) {
  return __builtin_amdgcn_mfma_f32_16x16x32_bf16(__builtin_bit_cast(bf16x8, a), __builtin_bit_cast(bf16x8, b), c, 0, 0, 0);
}
#define STAMP(i) do {} while (0)

template <int N>
__device__ __forceinline__ void gather(rsrc_t rs, const int (&off)[N], u32x4 (&v)[N], unsigned tA, unsigned tB, bool need_last) {
  for (unsigned spins = 0; spins < kSpin; ++spins) {
    asm volatile("" ::: "memory");
    bool ok = true;
#pragma unroll
    for (int i = 0; i < N; ++i) v[i] = ld_sc1(rs, off[i]);
#pragma unroll
    for (int i = 0; i < N; ++i) { const bool g = (v[i].y == tA) & (v[i].w == tB); ok &= (i == N - 1) ? (g | !need_last) : g; }
    if (__builtin_amdgcn_ballot_w64(!ok) == 0ull) break;
  }
}

__device__ __forceinline__ u32x4 bfrag576(LAS unsigned char* L, int base, int tbase, int T, int ks, int lane) {
  const int g = lane >> 4, q = (lane & 15) >> 2, p = lane & 3;
  const int r0 = 32 * ks + 4 * g + q, r1 = r0 + 16;
  int a0, a1;
  if (T < 32) { a0 = base + r0 * PITCH + T * 32 + p * 8; a1 = a0 + 16 * PITCH; }
  else { a0 = tbase + r0 * 128 + (((T - 32) ^ ((r0 >> 1) & 3)) * 32) + p * 8; a1 = tbase + r1 * 128 + (((T - 32) ^ ((r1 >> 1) & 3)) * 32) + p * 8; }
  const u32x2 x = trread(L + a0), y = trread(L + a1);
  return u32x4{x.x, x.y, y.x, y.y};
}
__device__ __forceinline__ u32x4 afrag(LAS unsigned char* L, int base_b0, int bstride, int ks, int lane) {
  const int row = lane & 15, g = lane >> 4;
  LAS unsigned char* p = L + base_b0 + (row & 1) * bstride + ks * 64 + g * 8;
  const u32x2 x = *(LAS u32x2*)p, y = *(LAS u32x2*)(p + 32);
  const bool v = row < 2;
  return u32x4{v ? x.x : 0u, v ? x.y : 0u, v ? y.x : 0u, v ? y.y : 0u};
}

__global__ void __launch_bounds__(NTHR, 2)
fused_block(const u16* __restrict__ x, const u16* __restrict__ kc, const u16* __restrict__ vc, const u16* __restrict__ wq, const u16* __restrict__ wo,
            const u16* __restrict__ wg, const u16* __restrict__ wu, const u16* __restrict__ wd, u16* __restrict__ out, unsigned char* __restrict__ ws) {
  extern __shared__ __attribute__((aligned(16))) unsigned char smem[];
  LAS unsigned char* L = (LAS unsigned char*)smem;
  const int tid = threadIdx.x, lane = tid & 63, wave = __builtin_amdgcn_readfirstlane(tid >> 6);
  const int blk = blockIdx.x, h = blk >> 3, j = blk & 7, b = j >> 2, s = j & 3;
  const int rr = (s - h) & 3, qd = b * 32 + h;
  const bool special = ((h >> 2) & 3) == 3;
  const bool is_wo = rr == 0, is_ffn = rr == 1 || rr == 2 || (rr == 3 && special);
  const int fidx = rr == 1 ? 2 * qd : rr == 2 ? 2 * (b * 32 + ((h + 1) & 31)) + 1 : 128 + 2 * (b * 4 + (h & 3)) + (h >> 4);
  const int rho = (rr == 3 && !special) ? b * 24 + (h - 4 * (h >> 4)) : (is_wo && qd < 24) ? 48 + qd : -1;
  STAMP(0);
  const rsrc_t rws = __builtin_amdgcn_make_buffer_rsrc((void*)ws, 0, WS_END, 0x00020000);
  const unsigned long long ep = __hip_atomic_load((GAS unsigned long long*)(ws + WS_EPOCH), __ATOMIC_RELAXED, __HIP_MEMORY_SCOPE_AGENT);

  u32x4 xa[2]; unsigned xb[2];
  {
    const unsigned char* xp = (const unsigned char*)x + ((18 * lane) & ~3);
#pragma unroll
    for (int bb = 0; bb < 2; ++bb) { xa[bb] = *(const GAS u32x4 __attribute__((aligned(4)))*)(xp + bb * 1152); xb[bb] = *(const GAS unsigned*)(xp + bb * 1152 + 16); }
  }
  unsigned wv[9];
  {
    const GAS unsigned* wp = (const GAS unsigned*)(wq + (size_t)(72 * j + 9 * wave) * (H * DH) + h * DH) + lane;
#pragma unroll
    for (int i = 0; i < 9; ++i) wv[i] = __builtin_nontemporal_load(wp + i * (H * DH / 2));
  }
  const rsrc_t rk = __builtin_amdgcn_make_buffer_rsrc((void*)(kc + (size_t)qd * SK * DH), 0, SK * DH * 2, 0x00020000);
  const rsrc_t rv = __builtin_amdgcn_make_buffer_rsrc((void*)(vc + (size_t)qd * SK * DH), 0, SK * DH * 2, 0x00020000);
  constexpr int NBIG = (CPQ + WO_LESS * CMUL) / 4, NSMALL = CPQ - 3 * NBIG;
  const int swo = h & 3;
  const int nch = is_wo ? NSMALL : NBIG;
  const int c0 = NBIG * s - (s > swo ? NBIG - NSMALL : 0);
  const int rot = (blk * SKEW * CMUL) % nch;
  auto choff = [&](int c) -> int { int p = c + rot; p = p >= nch ? p - nch : p; return c < nch ? (c0 + p) * CBYTES : 0x40000000; };
  u32x4 K[TPC], V[TPC];
  {
    const int vo = choff(wave) + lane * 16;
#pragma unroll
    for (int t = 0; t < TPC; ++t) K[t] = __builtin_amdgcn_raw_buffer_load_b128(rk, vo + t * 1024, 0, 2);
#pragma unroll
    for (int t = 0; t < TPC; ++t) V[t] = __builtin_amdgcn_raw_buffer_load_b128(rv, vo + t * 1024, 0, 2);
  }
  __builtin_amdgcn_sched_barrier(0);
  if (tid == 0) *(LAS int*)(L + L_CTR) = 8;

  {
    float acc[2][2];
#pragma unroll
    for (int bb = 0; bb < 2; ++bb) {
      unsigned d0 = xa[bb].x, d1 = xa[bb].y, d2 = xa[bb].z, d3 = xa[bb].w, d4 = xb[bb];
      if (lane & 1) { d0 = (d0 >> 16) | (d1 << 16); d1 = (d1 >> 16) | (d2 << 16); d2 = (d2 >> 16) | (d3 << 16); d3 = (d3 >> 16) | (d4 << 16); d4 >>= 16; }
      float xv[9] = {lo16(d0), hi16(d0), lo16(d1), hi16(d1), lo16(d2), hi16(d2), lo16(d3), hi16(d3), lo16(d4)};
      float sm = 0.f;
#pragma unroll
      for (int i = 0; i < 9; ++i) sm += xv[i];
      const float mean = wave_sum(sm) * (1.0f / D);
      float sq = 0.f;
#pragma unroll
      for (int i = 0; i < 9; ++i) { const float d = xv[i] - mean; sq = fmaf(d, d, sq); }
      const float var = rbf(wave_sum(sq) * (1.0f / D)), mb = rbf(mean);
      const float den = rbf(sqrtf(rbf(var + 1e-5f))), rden = 1.0f / den;
      float a0 = 0.f, a1 = 0.f;
#pragma unroll
      for (int i = 0; i < 9; ++i) {
        const float xn = rbf(fdiv(rbf(xv[i] - mb), den, rden));
        const float xs = __uint_as_float(__builtin_amdgcn_readlane(__float_as_uint(xn), 8 * j + wave));
        a0 = fmaf(xs, lo16(wv[i]), a0); a1 = fmaf(xs, hi16(wv[i]), a1);
      }
      acc[bb][0] = a0; acc[bb][1] = a1;
    }
    LAS f32x2* red = (LAS f32x2*)(L + L_RED);
    red[(wave * 2 + 0) * 64 + lane] = f32x2{acc[0][0], acc[0][1]};
    red[(wave * 2 + 1) * 64 + lane] = f32x2{acc[1][0], acc[1][1]};
  }
  const unsigned tA = __builtin_amdgcn_readfirstlane((unsigned)ep + 0x9E3779B1u);
  const unsigned tB = __builtin_amdgcn_readfirstlane((unsigned)(ep >> 32) + 0x85EBCA77u);
  BAR();
  if (wave < 2) {
    LAS f32x2* red = (LAS f32x2*)(L + L_RED);
    f32x2 t = red[wave * 64 + lane];
#pragma unroll
    for (int w = 1; w < 8; ++w) t += red[(w * 2 + wave) * 64 + lane];
    st_sc1(rws, WS_QP + ((h * 8 + j) * 2 + wave) * 1024 + lane * 16, u32x4{__float_as_uint(t.x), tA, __float_as_uint(t.y), tB});
  }
  STAMP(1);
  if (wave == 0) {
    int off[8]; u32x4 v[8];
#pragma unroll
    for (int p = 0; p < 8; ++p) off[p] = WS_QP + ((h * 8 + p) * 2 + b) * 1024 + lane * 16;
    gather<8>(rws, off, v, tA, tB, true);
    float q0 = 0.f, q1 = 0.f;
#pragma unroll
    for (int p = 0; p < 8; ++p) { q0 += __uint_as_float(v[p].x); q1 += __uint_as_float(v[p].z); }
    ((LAS unsigned*)(L + L_Q16))[lane] = cvtpk(q0, q1);
  }
  BAR();
  STAMP(2);
  if (is_wo) {
    const unsigned char* wb = (const unsigned char*)(wo + (size_t)(h * DH + b * 64) * D);
#pragma unroll
    for (int i = 0; i < 8; ++i) dma16(wb + (wave * 8 + i) * 1152 + lane * 16, L_WO + (wave * 8 + i) * PITCH);
    const int row = wave * 8 + (lane >> 3);
    dma16(wb + row * 1152 + 1024 + (((lane & 7) ^ (2 * ((row >> 1) & 3))) * 16), L_WOT + wave * 1024);
  }
  const u32x4 qp = *(LAS u32x4*)(L + L_Q16 + (lane & 15) * 16);

  constexpr float SCALE = 0.088388347648318440f, LOG2E = 1.4426950408889634f;
  float m = -INFINITY, l = 0.f, acc[8];
#pragma unroll
  for (int k = 0; k < 8; ++k) acc[k] = 0.f;
  int nxt;
  do {
    int c = 0;
    if (lane == 0) c = __hip_atomic_fetch_add((LAS int*)(L + L_CTR), 1, __ATOMIC_RELAXED, __HIP_MEMORY_SCOPE_WORKGROUP);
    nxt = __builtin_amdgcn_readfirstlane(c);
    const int vo = choff(nxt) + lane * 16;
    float sc[TPC];
#pragma unroll
    for (int t = 0; t < TPC; ++t) {
      float d = dot2(K[t].x, qp.x, 0.f); d = dot2(K[t].y, qp.y, d); d = dot2(K[t].z, qp.z, d); d = dot2(K[t].w, qp.w, d);
      asm volatile("" : "+v"(d));
      K[t] = __builtin_amdgcn_raw_buffer_load_b128(rk, vo + t * 1024, 0, 2);
      __builtin_amdgcn_sched_barrier(0);
      sc[t] = row_sum(d);
    }
    float pm = m;
#pragma unroll
    for (int t = 0; t < TPC; t += 2) {
      const unsigned r1 = cvtpk(sc[t], sc[t + 1]);
      const unsigned r2 = cvtpk(lo16(r1) * SCALE, hi16(r1) * SCALE);
      sc[t] = lo16(r2); sc[t + 1] = hi16(r2);
      pm = fmaxf(pm, fmaxf(sc[t], sc[t + 1]));
    }
    const float alpha = __builtin_amdgcn_exp2f((m - pm) * LOG2E);
    m = pm; l *= alpha;
    const float mo = -pm * LOG2E;
#pragma unroll
    for (int k = 0; k < 8; ++k) acc[k] *= alpha;
#pragma unroll
    for (int t = 0; t < TPC; ++t) { sc[t] = __builtin_amdgcn_exp2f(fmaf(sc[t], LOG2E, mo)); l += sc[t]; }
#pragma unroll
    for (int t = 0; t < TPC; ++t) {
      const float p = sc[t];
      acc[0] = fmaf(p, lo16(V[t].x), acc[0]); acc[1] = fmaf(p, hi16(V[t].x), acc[1]);
      acc[2] = fmaf(p, lo16(V[t].y), acc[2]); acc[3] = fmaf(p, hi16(V[t].y), acc[3]);
      acc[4] = fmaf(p, lo16(V[t].z), acc[4]); acc[5] = fmaf(p, hi16(V[t].z), acc[5]);
      acc[6] = fmaf(p, lo16(V[t].w), acc[6]); acc[7] = fmaf(p, hi16(V[t].w), acc[7]);
      asm volatile("" : "+v"(acc[0]), "+v"(acc[1]), "+v"(acc[2]), "+v"(acc[3]), "+v"(acc[4]), "+v"(acc[5]), "+v"(acc[6]), "+v"(acc[7]));
      V[t] = __builtin_amdgcn_raw_buffer_load_b128(rv, vo + t * 1024, 0, 2);
      __builtin_amdgcn_sched_barrier(0);
    }
  } while (nxt < nch);
  STAMP(3);

  {
    const float M = x1632_max(m);
    const float w = __builtin_amdgcn_exp2f((m - M) * LOG2E);
    const float ls = x1632_sum(l * w);
#pragma unroll
    for (int k = 0; k < 8; ++k) acc[k] = x1632_sum(acc[k] * w);
    LAS float* mr = (LAS float*)(L + L_RED) + wave * 136;
    if (lane < 16) {
      *(LAS f32x4*)(mr + 8 * lane) = f32x4{acc[0], acc[1], acc[2], acc[3]};
      *(LAS f32x4*)(mr + 8 * lane + 4) = f32x4{acc[4], acc[5], acc[6], acc[7]};
      if (lane == 0) *(LAS f32x2*)(mr + 128) = f32x2{M, ls};
    }
  }
  VM0();
  BAR();
  if (wave == 0) {
    LAS float* mr = (LAS float*)(L + L_RED);
    f32x2 ml[8], a[8];
#pragma unroll
    for (int w = 0; w < 8; ++w) { ml[w] = *(LAS f32x2*)(mr + w * 136 + 128); a[w] = *(LAS f32x2*)(mr + w * 136 + 2 * lane); }
    float M = ml[0].x;
#pragma unroll
    for (int w = 1; w < 8; ++w) M = fmaxf(M, ml[w].x);
    float ls = 0.f, o0 = 0.f, o1 = 0.f;
#pragma unroll
    for (int w = 0; w < 8; ++w) { const float e = __builtin_amdgcn_exp2f((ml[w].x - M) * LOG2E); ls = fmaf(e, ml[w].y, ls); o0 = fmaf(e, a[w].x, o0); o1 = fmaf(e, a[w].y, o1); }
    const int base = WS_AP + (blk * 2 + (lane >> 5)) * 640;
    st_sc1(rws, base + (lane & 31) * 16, u32x4{__float_as_uint(o0), tA, __float_as_uint(o1), tB});
    if ((lane & 31) == 0) st_sc1(rws, base + 512, u32x4{__float_as_uint(M), tA, __float_as_uint(ls), tB});
  }
  STAMP(4);

  if (is_ffn) {
    {
      const int f0 = fidx * 32;
#pragma unroll
      for (int i = 0; i < 9; ++i) {
        const int pc = (wave & 3) * 9 + i, row = pc * 16 + (lane >> 2);
        const u16* src = (wave < 4 ? wg : wu) + (size_t)row * DF + f0 + ((lane & 3) ^ (2 * ((row >> 2) & 1))) * 8;
        dma16(src, (wave < 4 ? L_G : L_U) + pc * 1024);
      }
      const unsigned char* db = (const unsigned char*)(wd + (size_t)f0 * D);
#pragma unroll
      for (int i = 0; i < 4; ++i) dma16(db + (wave * 4 + i) * 1152 + lane * 16, L_D + (wave * 4 + i) * PITCH);
      if (wave < 4) { const int row = wave * 8 + (lane >> 3); dma16(db + row * 1152 + 1024 + (((lane & 7) ^ (2 * ((row >> 1) & 3))) * 16), L_DT + wave * 1024); }
    }
    VM0();
    BAR();
    STAMP(5);
    const int mt = wave >> 2, tl = (wave >> 1) & 1, kh = wave & 1;
    u32x4 bg[9], bd[5];
    {
      const int g = lane >> 4, q = (lane & 15) >> 2, p = lane & 3;
#pragma unroll
      for (int i = 0; i < 9; ++i) {
        const int r0 = 32 * (9 * kh + i) + 4 * g + q;
        const int a0 = (mt ? L_U : L_G) + r0 * 64 + ((tl ^ ((r0 >> 2) & 1)) * 32) + p * 8;
        const u32x2 u0 = trread(L + a0), u1 = trread(L + a0 + 1024);
        bg[i] = u32x4{u0.x, u0.y, u1.x, u1.y};
      }
#pragma unroll
      for (int i = 0; i < 4; ++i) bd[i] = bfrag576(L, L_D, L_DT, wave + 8 * i, 0, lane);
      bd[4] = bfrag576(L, L_D, L_DT, 32 + (wave & 3), 0, lane);
    }
    if (wave < 2) {
      int off[3]; u32x4 v[3];
      off[0] = WS_HP + wave * 2304 + lane * 16; off[1] = off[0] + 1024; off[2] = lane < 16 ? off[0] + 2048 : 0x40000000;
      gather<3>(rws, off, v, tA, tB, lane < 16);
      STAMP(6);
      float hv[12];
#pragma unroll
      for (int i = 0; i < 3; ++i) { hv[4 * i] = lo16(v[i].x); hv[4 * i + 1] = hi16(v[i].x); hv[4 * i + 2] = lo16(v[i].z); hv[4 * i + 3] = hi16(v[i].z); }
      float sm = 0.f;
#pragma unroll
      for (int i = 0; i < 12; ++i) sm += hv[i];
      const float mean = wave_sum(sm) * (1.0f / D);
      float sq = 0.f, sq2 = 0.f;
#pragma unroll
      for (int i = 0; i < 8; ++i) { const float d = hv[i] - mean; sq = fmaf(d, d, sq); }
#pragma unroll
      for (int i = 8; i < 12; ++i) { const float d = hv[i] - mean; sq2 = fmaf(d, d, sq2); }
      sq += lane < 16 ? sq2 : 0.f;
      const float var = rbf(wave_sum(sq) * (1.0f / D)), mb = rbf(mean);
      const float den = rbf(sqrtf(rbf(var + 1e-5f))), rden = 1.0f / den;
      LAS u32x2* xn = (LAS u32x2*)(L + L_XN + wave * 1152);
#pragma unroll
      for (int i = 0; i < 3; ++i) {
        const unsigned e0 = cvtpk(hv[4 * i] - mb, hv[4 * i + 1] - mb), e1 = cvtpk(hv[4 * i + 2] - mb, hv[4 * i + 3] - mb);
        const u32x2 o = {cvtpk(fdiv(lo16(e0), den, rden), fdiv(hi16(e0), den, rden)), cvtpk(fdiv(lo16(e1), den, rden), fdiv(hi16(e1), den, rden))};
        if (i < 2 || lane < 16) xn[i * 64 + lane] = o;
      }
    }
    BAR();
    {
      f32x4 c = {0.f, 0.f, 0.f, 0.f};
#pragma unroll
      for (int i = 0; i < 9; ++i) c = mfma(afrag(L, L_XN, 1152, 9 * kh + i, lane), bg[i], c);
      if (lane < 16) *(LAS f32x2*)(L + L_GU + wave * 128 + lane * 8) = f32x2{c[0], c[1]};
    }
    BAR();
    {
      const int bb = lane >> 5, col = lane & 31, t2 = col >> 4, ci = col & 15;
      LAS float* gu = (LAS float*)(L + L_GU) + ci * 2 + bb;
      const float g = rbf(gu[(2 * t2) * 32] + gu[(2 * t2 + 1) * 32]), u = rbf(gu[(4 + 2 * t2) * 32] + gu[(5 + 2 * t2) * 32]);
      const float inner = 0.7978845608028654f * fmaf(0.044715f * g * g, g, g);
      const float ge = rbf(g / (1.0f + __builtin_amdgcn_exp2f(-2.0f * LOG2E * inner)));
      ((LAS u16*)(L + L_ACT + wave * 128))[lane] = (u16)cvtpk(ge * u, 0.f);
      asm volatile("s_waitcnt lgkmcnt(0)" ::: "memory");
      const u32x4 a = afrag(L, L_ACT + wave * 128, 64, 0, lane);
      const f32x4 z = {0.f, 0.f, 0.f, 0.f};
#pragma unroll
      for (int i = 0; i < 5; ++i) {
        const f32x4 c = mfma(a, bd[i], z);
        const int T = i < 4 ? wave + 8 * i : 32 + wave;
        if (lane < 16 && (i < 4 || wave < 4))
          st_sc1(rws, WS_OP + ((2 * T + (lane >> 3)) * 144 + fidx) * 128 + (lane & 7) * 16, u32x4{__float_as_uint(c[0]), tA, __float_as_uint(c[1]), tB});
      }
    }
    STAMP(7);
    return;
  }

  u16 xr0 = 0, xr1 = 0;
  if (rho >= 0 && wave == 0) { xr0 = x[8 * rho + (lane & 7)]; xr1 = x[D + 8 * rho + (lane & 7)]; }

  if (is_wo) {
    u32x4 bw[5][2];
#pragma unroll
    for (int i = 0; i < 5; ++i)
#pragma unroll
      for (int ks = 0; ks < 2; ++ks) bw[i][ks] = bfrag576(L, L_WO, L_WOT, i < 4 ? wave + 8 * i : 32 + (wave & 3), ks, lane);
    if (wave == 0) {
      int off[8]; u32x4 v[8];
#pragma unroll
      for (int sp = 0; sp < 4; ++sp) {
        const int base = WS_AP + ((h * 8 + (lane >> 5) * 4 + sp) * 2 + b) * 640;
        off[sp] = base + (lane & 31) * 16; off[4 + sp] = base + 512;
      }
      gather<8>(rws, off, v, tA, tB, true);
      STAMP(5);
      float M = __uint_as_float(v[4].x);
#pragma unroll
      for (int sp = 1; sp < 4; ++sp) M = fmaxf(M, __uint_as_float(v[4 + sp].x));
      float ls = 0.f, o0 = 0.f, o1 = 0.f;
#pragma unroll
      for (int sp = 0; sp < 4; ++sp) {
        const float e = __builtin_amdgcn_exp2f((__uint_as_float(v[4 + sp].x) - M) * LOG2E);
        ls = fmaf(e, __uint_as_float(v[4 + sp].z), ls); o0 = fmaf(e, __uint_as_float(v[sp].x), o0); o1 = fmaf(e, __uint_as_float(v[sp].z), o1);
      }
      const float rl = 1.0f / ls;
      ((LAS unsigned*)(L + L_O16))[lane] = cvtpk(o0 * rl, o1 * rl);
    }
    BAR();
    const u32x4 a0 = afrag(L, L_O16, 128, 0, lane), a1 = afrag(L, L_O16, 128, 1, lane);
    const f32x4 z = {0.f, 0.f, 0.f, 0.f};
#pragma unroll
    for (int i = 0; i < 5; ++i) {
      const f32x4 c = mfma(a1, bw[i][1], mfma(a0, bw[i][0], z));
      const int T = i < 4 ? wave + 8 * i : 32 + wave;
      if (lane < 16 && (i < 4 || wave < 4))
        st_sc1(rws, WS_YP + ((2 * T + (lane >> 3)) * 64 + qd) * 128 + (lane & 7) * 16, u32x4{__float_as_uint(c[0]), tA, __float_as_uint(c[1]), tB});
    }
    STAMP(6);
  }

  if (rho >= 0 && wave == 0) {
    float h0, h1;
    {
      int off[8]; u32x4 v[8];
#pragma unroll
      for (int i = 0; i < 8; ++i) off[i] = WS_YP + rho * 8192 + i * 1024 + lane * 16;
      gather<8>(rws, off, v, tA, tB, true);
      STAMP(8);
      float y0 = 0.f, y1 = 0.f;
#pragma unroll
      for (int i = 0; i < 8; ++i) { y0 += __uint_as_float(v[i].x); y1 += __uint_as_float(v[i].z); }
      y0 += dpp<XOR8>(y0); y1 += dpp<XOR8>(y1);
      y0 = x1632_sum(y0); y1 = x1632_sum(y1);
      h0 = rbf(lo16(xr0) + rbf(y0)); h1 = rbf(lo16(xr1) + rbf(y1));
      const unsigned hb = __float_as_uint((lane & 8) ? h1 : h0) >> 16;
      const unsigned pk = hb | (dppu<XOR1>(hb) << 16);
      const unsigned pk2 = dppu<0xAA>(pk);
      if (lane < 16 && (lane & 3) == 0) st_sc1(rws, WS_HP + (lane >> 3) * 2304 + (2 * rho + ((lane >> 2) & 1)) * 16, u32x4{pk, tA, pk2, tB});
      STAMP(9);
    }
    {
      int off[18]; u32x4 v[18];
#pragma unroll
      for (int i = 0; i < 18; ++i) off[i] = WS_OP + rho * 18432 + i * 1024 + lane * 16;
      gather<18>(rws, off, v, tA, tB, true);
      STAMP(10);
      float y0 = 0.f, y1 = 0.f;
#pragma unroll
      for (int i = 0; i < 18; ++i) { y0 += __uint_as_float(v[i].x); y1 += __uint_as_float(v[i].z); }
      y0 += dpp<XOR8>(y0); y1 += dpp<XOR8>(y1);
      y0 = x1632_sum(y0); y1 = x1632_sum(y1);
      const float r = (lane & 8) ? h1 + rbf(y1) : h0 + rbf(y0);
      if (lane < 16) out[(lane >> 3) * D + 8 * rho + (lane & 7)] = (u16)cvtpk(r, r);
      if (rho == 0 && lane == 0)
        __hip_atomic_store((GAS unsigned long long*)(ws + WS_EPOCH), (unsigned long long)tA | ((unsigned long long)tB << 32), __ATOMIC_RELAXED, __HIP_MEMORY_SCOPE_AGENT);
      STAMP(11);
    }
  }
}

extern "C" void kernel_launch(void* const* d_in, const int* in_sizes, int n_in, void* d_out, int out_size, void* d_ws, size_t ws_size, hipStream_t stream) {
  (void)hipFuncSetAttribute((const void*)fused_block, hipFuncAttributeMaxDynamicSharedMemorySize, L_END);
  fused_block<<<NWG, NTHR, L_END, stream>>>((const u16*)d_in[0], (const u16*)d_in[1], (const u16*)d_in[2], (const u16*)d_in[3], (const u16*)d_in[4],
                                           (const u16*)d_in[6], (const u16*)d_in[7], (const u16*)d_in[8], (u16*)d_out, (unsigned char*)d_ws);
}
```

```cpp
#include <hip/hip_runtime.h>
#include <stdint.h>

#define TGT 0

typedef unsigned short u16;
typedef unsigned u32x4 __attribute__((ext_vector_type(4)));
typedef unsigned u32x2 __attribute__((ext_vector_type(2)));
typedef float f32x4 __attribute__((ext_vector_type(4)));
typedef float f32x2 __attribute__((ext_vector_type(2)));
typedef short s16x4 __attribute__((ext_vector_type(4)));
typedef __bf16 bf16x2 __attribute__((ext_vector_type(2)));
typedef __bf16 bf16x8 __attribute__((ext_vector_type(8)));
typedef __amdgpu_buffer_rsrc_t rsrc_t;
#define LAS __attribute__((address_space(3)))

constexpr int D = 576, H = 32, SK = 8192, DH = 128, DF = 4608;
constexpr int NWG = 256, NTHR = 512;
constexpr int TPC = 4, CBYTES = 4096, NCH = 128;
constexpr unsigned kSpin = 1u << 17;
constexpr int OOB = 0x40000000;
constexpr int QDELAY = 35;
constexpr int GL = 38, GLX = 30;
constexpr int TEND = 3640, SLOPE = 133 * 256 / 5;
constexpr int STAG = 40;

constexpr int HRS = 73 * 128, YTS = 65 * 128, OTS = 145 * 128;
constexpr int WS_EPOCH = 0;
constexpr int WS_HP = 4096;
constexpr int WS_AP = 81920;
constexpr int WS_QP = WS_AP + 256 * 2 * 384;
constexpr int WS_YP = WS_QP + 32 * 8 * 2 * 1024;
constexpr int WS_OP = WS_YP + 36 * YTS;
constexpr int WS_END = WS_OP + 36 * OTS;

constexpr int PITCH = 1056;
constexpr int L_WO = 0, L_WOT = 64 * PITCH;
constexpr int L_G = 0, L_U = 36864, L_D = 73728, L_DT = L_D + 32 * PITCH;
constexpr int L_RED = L_DT + 32 * 128;
constexpr int L_Q16 = L_RED + 4096;
constexpr int L_O16 = L_Q16 + 256;
constexpr int L_XN = L_O16 + 256;
constexpr int L_GU = L_XN + 2304;
constexpr int L_ACT = L_GU + 1024;
constexpr int L_ML = L_ACT + 1024;
constexpr int L_TRASH = L_ML + 64;
constexpr int L_CTR = L_TRASH + 512;
constexpr int L_END = L_CTR + 64;
static_assert(L_WOT + 64 * 128 <= L_RED && L_END > 80 * 1024, "one workgroup per CU");
enum { F_QCNT, F_QRDY, F_POLL, F_T0, F_LAND };

__device__ __forceinline__ float lo16(unsigned u) { return __uint_as_float(u << 16); }
__device__ __forceinline__ float hi16(unsigned u) { return __uint_as_float(u & 0xffff0000u); }
__device__ __forceinline__ unsigned cvtpk(float lo, float hi) {
  bf16x2 r; r.x = (__bf16)lo; r.y = (__bf16)hi; return __builtin_bit_cast(unsigned, r);
}
__device__ __forceinline__ unsigned cvtpk_a(float lo, float hi) {
  unsigned r; asm("v_cvt_pk_bf16_f32 %0, %1, %2" : "=v"(r) : "v"(lo), "v"(hi)); return r;
}
__device__ __forceinline__ float rbf(float f) { return lo16(cvtpk(f, f)); }
__device__ __forceinline__ float fmx(float a, float b) {
  float r; asm("v_max_f32 %0, %1, %2" : "=v"(r) : "v"(a), "v"(b)); return r;
}
__device__ __forceinline__ float fmx3(float a, float b, float c) {
  float r; asm("v_max3_f32 %0, %1, %2, %3" : "=v"(r) : "v"(a), "v"(b), "v"(c)); return r;
}
__device__ __forceinline__ float max8(float x) {
  float r;
  asm("s_nop 1\n\tv_max_f32_dpp %0, %1, %1 row_half_mirror row_mask:0xf bank_mask:0xf\n\ts_nop 1\n\t"
      "v_max_f32_dpp %0, %0, %0 quad_perm:[2,3,0,1] row_mask:0xf bank_mask:0xf\n\ts_nop 1\n\t"
      "v_max_f32_dpp %0, %0, %0 quad_perm:[1,0,3,2] row_mask:0xf bank_mask:0xf" : "=&v"(r) : "v"(x));
  return r;
}
__device__ __forceinline__ float dot2(unsigned k, unsigned q, float acc) {
  return __builtin_amdgcn_fdot2_f32_bf16(__builtin_bit_cast(bf16x2, k), __builtin_bit_cast(bf16x2, q), acc, false);
}
template <int CTRL> __device__ __forceinline__ float dpp(float x) {
  return __builtin_bit_cast(float, __builtin_amdgcn_mov_dpp(__builtin_bit_cast(int, x), CTRL, 0xf, 0xf, true));
}
template <int CTRL> __device__ __forceinline__ unsigned dppu(unsigned x) {
  return (unsigned)__builtin_amdgcn_mov_dpp((int)x, CTRL, 0xf, 0xf, true);
}
constexpr int XOR1 = 0xB1, XOR2 = 0x4E, XOR7 = 0x141, XOR8 = 0x128, ROR4 = 0x124;
__device__ __forceinline__ float pair32(float a, float b) {
  auto s = __builtin_amdgcn_permlane32_swap(__float_as_uint(a), __float_as_uint(b), false, false);
  return __uint_as_float(s[0]) + __uint_as_float(s[1]);
}
__device__ __forceinline__ float pair16(float a, float b) {
  auto s = __builtin_amdgcn_permlane16_swap(__float_as_uint(a), __float_as_uint(b), false, false);
  return __uint_as_float(s[0]) + __uint_as_float(s[1]);
}
__device__ __forceinline__ float x16_max(float x) {
  auto s = __builtin_amdgcn_permlane16_swap(__float_as_uint(x), __float_as_uint(x), false, false);
  return fmx(__uint_as_float(s[0]), __uint_as_float(s[1]));
}
__device__ __forceinline__ float x32_max(float x) {
  auto s = __builtin_amdgcn_permlane32_swap(__float_as_uint(x), __float_as_uint(x), false, false);
  return fmx(__uint_as_float(s[0]), __uint_as_float(s[1]));
}
__device__ __forceinline__ float row_sum(float d) { d += dpp<XOR8>(d); d += dpp<XOR7>(d); d += dpp<XOR2>(d); d += dpp<XOR1>(d); return d; }
__device__ __forceinline__ float fdiv(float a, float b, float rb) {
  const float q = a * rb; return fmaf(fmaf(-q, b, a), rb, q);
}
#define BAR() asm volatile("s_waitcnt lgkmcnt(0)\n\ts_barrier" ::: "memory")
#define LGKM0() asm volatile("s_waitcnt lgkmcnt(0)" ::: "memory")
#define FENCE() asm volatile("" ::: "memory")
#define PIN(x) asm volatile("" : "+v"(x))
#define SCHED() __builtin_amdgcn_sched_barrier(0)
__device__ __forceinline__ void dma16(const void* gsrc, unsigned lds_dst) {
  unsigned keep;
  asm volatile("s_mov_b32 %0, m0\n\ts_mov_b32 m0, %2\n\ts_nop 0\n\tglobal_load_lds_dwordx4 %1, off nt\n\ts_mov_b32 m0, %0" : "=&s"(keep) : "v"(gsrc), "s"(lds_dst) : "memory");
}
__device__ __forceinline__ rsrc_t mkrsrc(const void* p, int bytes) { return __builtin_amdgcn_make_buffer_rsrc((void*)p, 0, bytes, 0x00020000); }
__device__ __forceinline__ u32x4 ld_sc1(rsrc_t r, int off) { return __builtin_amdgcn_raw_buffer_load_b128(r, off, 0, 16); }
__device__ __forceinline__ void st_sc1(rsrc_t r, int off, u32x4 v) { __builtin_amdgcn_raw_buffer_store_b128(v, r, off, 0, 16); }
__device__ __forceinline__ u32x4 ld_nt(rsrc_t r, int off) { return __builtin_amdgcn_raw_buffer_load_b128(r, off, 0, 2); }
__device__ __forceinline__ u32x2 trread(LAS unsigned char* p) {
  return __builtin_bit_cast(u32x2, __builtin_amdgcn_ds_read_tr16_b64_v4i16((LAS s16x4*)p));
}
__device__ __forceinline__ f32x4 mfma(u32x4 a, u32x4 b, f32x4 c) {
  return __builtin_amdgcn_mfma_f32_16x16x32_bf16(__builtin_bit_cast(bf16x8, a), __builtin_bit_cast(bf16x8, b), c, 0, 0, 0);
}

struct Tags { unsigned a, b, p0, p1, p2, p3; };
__device__ __forceinline__ u32x4 pack4(float v0, float v1, float v2, float v3, unsigned km, const Tags& t) {
  return u32x4{(__float_as_uint(v0) & km) | t.p0, (__float_as_uint(v1) & km) | t.p1, (__float_as_uint(v2) & km) | t.p2, (__float_as_uint(v3) & km) | t.p3};
}
__device__ __forceinline__ unsigned orxor(unsigned acc, unsigned x, unsigned t) { return __builtin_amdgcn_bitop3_b32(acc, x, t, 0xF6); }
template <int N, bool PACKED>
__device__ __forceinline__ void gather(rsrc_t rs, const int (&off)[N], u32x4 (&v)[N], const Tags& t, bool last = true) {
  for (unsigned spins = 0; spins < kSpin; ++spins) {
    FENCE();
#pragma unroll
    for (int i = 0; i < N; ++i) v[i] = ld_sc1(rs, off[i]);
    unsigned bad = 0;
#pragma unroll
    for (int i = 0; i < N; ++i) {
      if (i == N - 1 && N == 3) break;
      if (PACKED) { v[i].x ^= t.p0; v[i].y ^= t.p1; v[i].z ^= t.p2; v[i].w ^= t.p3; bad |= (v[i].x | v[i].y) | (v[i].z | v[i].w); }
      else bad = orxor(orxor(bad, v[i].y, t.a), v[i].w, t.b);
    }
    if (N == 3) bad |= last ? (v[N - 1].y ^ t.a) | (v[N - 1].w ^ t.b) : 0u;
    if (__builtin_amdgcn_ballot_w64((PACKED ? bad << 20 : bad) != 0u) == 0ull) break;
  }
}
template <bool TAIL>
__device__ __forceinline__ u32x4 bfrag576(LAS unsigned char* L, int base, int T, int ks, int lane) {
  const int g = lane >> 4, q = (lane & 15) >> 2, p = lane & 3;
  const int r0 = 32 * ks + 8 * g + q;
  u32x2 x, y;
  if (!TAIL) { LAS unsigned char* a = L + base + r0 * PITCH + T * 32 + p * 8; x = trread(a); y = trread(a + 4 * PITCH); }
  else { x = trread(L + base + r0 * 128 + ((T ^ ((r0 >> 1) & 3)) * 32) + p * 8); y = trread(L + base + (r0 + 4) * 128 + ((T ^ (((r0 + 4) >> 1) & 3)) * 32) + p * 8); }
  return u32x4{x.x, x.y, y.x, y.y};
}
template <int NT>
__device__ __forceinline__ void publish_tiles(rsrc_t rws, const f32x4 (&c)[NT], int o4, int o1, unsigned km, int lane, const Tags& t) {
  const bool g1 = lane & 16, g2 = lane & 32;
  const float a0 = g1 ? c[1][0] : c[0][0], b0 = g1 ? c[3][0] : c[2][0], a1 = g1 ? c[1][1] : c[0][1], b1 = g1 ? c[3][1] : c[2][1];
  const float s0 = g2 ? b0 : a0, s1 = g2 ? b1 : a1;
  st_sc1(rws, o4, pack4(s0, s1, dpp<XOR1>(s0), dpp<XOR1>(s1), km, t));
  const float r0 = g1 ? c[NT - 1][0] : c[4][0], r1 = g1 ? c[NT - 1][1] : c[4][1];
  st_sc1(rws, o1, pack4(r0, r1, dpp<XOR1>(r0), dpp<XOR1>(r1), km, t));
}
template <int N>
__device__ __forceinline__ float reduce_units(const u32x4 (&v)[N]) {
  f32x4 s = __builtin_bit_cast(f32x4, v[0]), s2 = __builtin_bit_cast(f32x4, v[1]);
#pragma unroll
  for (int i = 2; i < N; ++i) { if (i & 1) s2 += __builtin_bit_cast(f32x4, v[i]); else s += __builtin_bit_cast(f32x4, v[i]); }
  s += s2;
  float y = pair16(pair32(s[0], s[2]), pair32(s[1], s[3]));
  y += dpp<XOR8>(y);
  return y + dpp<ROR4>(y);
}

__global__ void __launch_bounds__(NTHR, 2)
fused_block(const u16* __restrict__ x, const u16* __restrict__ kc, const u16* __restrict__ vc, const u16* __restrict__ wq, const u16* __restrict__ wo,
            const u16* __restrict__ wg, const u16* __restrict__ wu, const u16* __restrict__ wd, u16* __restrict__ out, unsigned char* __restrict__ ws) {
  extern __shared__ __attribute__((aligned(16))) unsigned char smem[];
  LAS unsigned char* L = (LAS unsigned char*)smem;
  const int tid = threadIdx.x, lane = tid & 63, wave = __builtin_amdgcn_readfirstlane(tid >> 6);
  const int blk = blockIdx.x, h = blk >> 3, j = blk & 7, b = j >> 2, s = j & 3;
  asm volatile("" :: "s"(x), "s"(kc), "s"(vc), "s"(wq), "s"(ws));
  const rsrc_t rws = mkrsrc(ws, WS_END);
  u32x2 ep;
  Tags tg;
  auto tags = [&]() {
    PIN(ep);
    tg.a = __builtin_amdgcn_readfirstlane(ep.x) + 0x9E3779B1u; tg.b = __builtin_amdgcn_readfirstlane(ep.y) + 0x85EBCA77u;
    tg.p0 = tg.a & 0xfffu; tg.p1 = (tg.a >> 12) & 0xfffu; tg.p2 = tg.b & 0xfffu; tg.p3 = (tg.b >> 12) & 0xfffu;
  };
  volatile LAS int* flags = (volatile LAS int*)(L + L_CTR);
  auto flag = [&](int i) { return __builtin_amdgcn_readfirstlane(flags[i]); };
  auto lds_inc = [&](int i) { if (lane == 0) __hip_atomic_fetch_add((LAS int*)(L + L_CTR) + i, 1, __ATOMIC_RELAXED, __HIP_MEMORY_SCOPE_WORKGROUP); };
  u32x4 qv[8];
  const int qo = WS_QP + (h * 16 + b) * 1024;
  if (wave < 4) {
    ep = __builtin_amdgcn_raw_buffer_load_b64(rws, WS_EPOCH, 0, 16);
    BAR();
  } else {
    __builtin_amdgcn_s_setprio(3);
    if (lane < 16) ((LAS int*)(L + L_CTR))[lane] = 0;
    const rsrc_t rx = mkrsrc(x, 2 * D * 2), rq = mkrsrc(wq + h * DH, D * H * DH * 2 - h * DH * 2);
    const u32x4 xa = __builtin_amdgcn_raw_buffer_load_b128(rx, lane * 36, 0, 0);
    const u32x4 xb = __builtin_amdgcn_raw_buffer_load_b128(rx, lane * 36 + 16, 0, 0);
    const unsigned xc = __builtin_amdgcn_raw_buffer_load_b32(rx, lane * 36 + 32, 0, 0);
    const unsigned xq = __builtin_amdgcn_raw_buffer_load_b32(rx, (lane >> 5) * 1152 + ((lane & 15) < 8 ? lane & 15 : 8) * 4, (36 * j + 9 * (wave - 4)) * 4, 0);
    const int r0 = (72 * j + 18 * (wave - 4)) * (H * DH * 2);
    unsigned wv[18];
#pragma unroll
    for (int i = 0; i < 18; ++i) wv[i] = __builtin_amdgcn_raw_buffer_load_b32(rq, lane * 4, r0 + i * (H * DH * 2), 2);
    ep = __builtin_amdgcn_raw_buffer_load_b64(rws, WS_EPOCH, 0, 16);
    SCHED();
    BAR();
    if (wave == 4) flags[F_T0] = (int)(unsigned)__builtin_amdgcn_s_memrealtime();
    SCHED();
    LAS f32x2* red = (LAS f32x2*)(L + L_RED);
    const unsigned xd[9] = {xa.x, xa.y, xa.z, xa.w, xb.x, xb.y, xb.z, xb.w, xc};
    float s1 = 0.f, s2 = 0.f;
#pragma unroll
    for (int i = 0; i < 9; ++i) { s1 = dot2(xd[i], 0x3f803f80u, s1); s2 = dot2(xd[i], xd[i], s2); }
    const float st = row_sum(pair16(s1, s2)) * (1.0f / D);
    auto sw = __builtin_amdgcn_permlane16_swap(__float_as_uint(st), __float_as_uint(st), false, false);
    const float mean = __uint_as_float(sw[0]), msq = __uint_as_float(sw[1]);
    const unsigned mv = cvtpk_a(mean, fmx(fmaf(-mean, mean, msq), 0.f));
    const float den = rbf(__builtin_amdgcn_sqrtf(rbf(hi16(mv) + 1e-5f))), rden = __builtin_amdgcn_rcpf(den);
    const unsigned e = cvtpk_a(lo16(xq) - lo16(mv), hi16(xq) - lo16(mv));
    const unsigned xn = cvtpk_a(fdiv(lo16(e), den, rden), fdiv(hi16(e), den, rden));
    float a00 = 0.f, a01 = 0.f, a10 = 0.f, a11 = 0.f;
#pragma unroll
    for (int i = 0; i < 9; ++i) {
      const unsigned wl = __builtin_amdgcn_perm(wv[2 * i + 1], wv[2 * i], 0x05040100u), wh = __builtin_amdgcn_perm(wv[2 * i + 1], wv[2 * i], 0x07060302u);
      const unsigned x0 = __builtin_amdgcn_readlane(xn, i), x1 = __builtin_amdgcn_readlane(xn, 32 + i);
      a00 = dot2(wl, x0, a00); a01 = dot2(wh, x0, a01); a10 = dot2(wl, x1, a10); a11 = dot2(wh, x1, a11);
    }
    red[((wave - 4) * 2 + 0) * 64 + lane] = f32x2{a00, a01};
    red[((wave - 4) * 2 + 1) * 64 + lane] = f32x2{a10, a11};
    tags();
    LGKM0();
    lds_inc(F_QCNT);
    if (wave < 6) {
      while (flag(F_QCNT) < 4) {}
      FENCE();
      f32x2 t = red[(wave - 4) * 64 + lane];
#pragma unroll
      for (int w = 1; w < 4; ++w) t += red[(w * 2 + wave - 4) * 64 + lane];
      st_sc1(rws, WS_QP + ((h * 8 + j) * 2 + wave - 4) * 1024 + lane * 16, u32x4{__float_as_uint(t.x), tg.a, __float_as_uint(t.y), tg.b});
    }
    if (wave == 4) {
      __builtin_amdgcn_s_sleep(QDELAY);
      FENCE();
#pragma unroll
      for (int p = 0; p < 8; ++p) qv[p] = __builtin_amdgcn_raw_buffer_load_b128(rws, lane * 16, qo + p * 2048, 16);
      FENCE();
      flags[F_POLL] = 1;
    } else {
      __builtin_amdgcn_s_setprio(0);
      if (wave == 7) { while (flag(F_POLL) == 0) __builtin_amdgcn_s_sleep(1); }
    }
  }
  SCHED();
  const size_t kvo = (size_t)(b * 32 + h) * SK * DH;
  const rsrc_t rk = mkrsrc(kc + kvo, SK * DH * 2), rv = mkrsrc(vc + kvo, SK * DH * 2);
  int vo = (s * 8 + wave) * CBYTES + lane * 16 + ((h & 15) << 17);
  u32x4 K[TPC];
#pragma unroll
  for (int t = 0; t < TPC; ++t) { K[t] = ld_nt(rk, vo + t * 1024); SCHED(); }
  if (wave == 4) {
    unsigned bad = 0;
#pragma unroll
    for (int p = 0; p < 8; ++p) bad = orxor(orxor(bad, qv[p].y, tg.a), qv[p].w, tg.b);
    if (__builtin_amdgcn_ballot_w64(bad != 0u) != 0ull) {
      int qoff[8];
#pragma unroll
      for (int p = 0; p < 8; ++p) qoff[p] = qo + p * 2048 + lane * 16;
      gather<8, false>(rws, qoff, qv, tg);
    }
    float q0 = 0.f, q1 = 0.f;
#pragma unroll
    for (int p = 0; p < 8; ++p) { q0 += __uint_as_float(qv[p].x); q1 += __uint_as_float(qv[p].z); }
    ((LAS unsigned*)(L + L_Q16))[lane] = cvtpk(q0, q1);
    LGKM0();
    flags[F_QRDY] = 1;
    __builtin_amdgcn_s_setprio(0);
  }
  SCHED();
  const int rr = (s - h) & 3, hq = (h >> 2) & 3;
  const bool is_wo = (rr == 0 && hq != 3) || (rr == 3 && hq == 2), is_ffn = rr == 1 || rr == 2 || (rr == 3 && hq == 3);
  const int hw = rr == 0 ? h : h + 4;
  const int fidx = rr == 1 ? 2 * (b * 32 + h) : rr == 2 ? 2 * (b * 32 + ((h + 1) & 31)) + 1 : 128 + 2 * (b * 4 + (h & 3)) + (h >> 4);
  const int gl = (((int)((size_t)wg >> 7) + (fidx >> 1)) & 7) == 3 ? GLX : GL;
  auto gu_dma = [&](int p0, int p1, int n) {
    const int sw = ((lane & 3) ^ (2 * ((lane >> 5) & 1))) * 8, r = lane >> 2;
    for (int p = p0 + wave; p < p1; p += n) {
      const int q = p >= 36 ? p - 36 : p;
      dma16((p >= 36 ? wu : wg) + (size_t)(q * 16 + r) * DF + fidx * 32 + sw, L_G + p * 1024);
    }
  };
  if (wave < 4) {
    if (is_wo) {
      const unsigned char* wb = (const unsigned char*)(wo + (size_t)(hw * DH + b * 64) * D);
#pragma unroll 4
      for (int i = 0; i < 16; ++i) dma16(wb + (wave * 16 + i) * 1152 + lane * 16, L_WO + (wave * 16 + i) * PITCH);
#pragma unroll
      for (int i = 0; i < 2; ++i) {
        const int row = wave * 16 + i * 8 + (lane >> 3);
        dma16(wb + row * 1152 + 1024 + (((lane & 7) ^ (2 * ((row >> 1) & 3))) * 16), L_WOT + (wave * 2 + i) * 1024);
      }
    } else if (is_ffn) {
      gu_dma(0, 72 - gl, 4);
    }
  }
  SCHED();
  tags();
  const int rho = (rr == 3 && hq < 2) ? b * 16 + (h >> 4) * 8 + (h & 3) * 2 + hq : (rr == 0 && hq == 3) ? 32 + b * 8 + (h & 3) * 2 + (h >> 4)
                : (rr == 0 && hq < 2 && (b == 0 || h < 8)) ? 48 + b * 16 + (h >> 4) * 8 + (h & 3) * 2 + hq : -1;
  const int rcol = (lane & 16) * (D / 16) + 8 * rho + 2 * (lane & 3) + (lane >> 5);
  const unsigned xr = __builtin_amdgcn_raw_buffer_load_b16(mkrsrc(x, 2 * D * 2), (rho >= 0 && wave == 0) ? rcol * 2 : OOB, 0, 0);
  while (flag(F_QRDY) == 0) __builtin_amdgcn_s_sleep(1);
  FENCE();
  const u32x4 qp = *(LAS u32x4*)(L + L_Q16 + (lane & 15) * 16);

  constexpr float SCALE = 0.088388347648318440f, LOG2E = 1.4426950408889634f;
  float m = -3.0e38f, l = 0.f, acc[8];
#pragma unroll
  for (int k = 0; k < 8; ++k) acc[k] = 0.f;
  int aoff = (lane & 30) * 8;
  aoff = (lane & 1) ? OOB : aoff;
  aoff = (lane & 31) == 1 ? 256 : aoff;
  aoff += WS_AP + (blk * 2 + (lane >> 5)) * 384;
  int a_mw = L_RED + wave * 512 + (lane & 15) * 32 + (lane >> 4) * 8, a_mr = L_RED + lane * 8, a_ml = L_ML + (lane & 7) * 8;
  unsigned km = 0xfffff000u;
  PIN(aoff); PIN(a_mw); PIN(a_mr); PIN(a_ml); PIN(km);
  const unsigned t0 = (unsigned)flag(F_T0);
  int send = TEND + ((((h * 13) & 31) * STAG) >> 5) - STAG / 2;
  [[maybe_unused]] const int send0 = send;
  for (int left = NCH - 8 - wave; left > -8; left -= 8) {
    unsigned now = (unsigned)__builtin_amdgcn_s_memrealtime();
    float sc[TPC];
#pragma unroll
    for (int t = 0; t < TPC; ++t) {
      float d = dot2(K[t].x, qp.x, 0.f); d = dot2(K[t].y, qp.y, d); d = dot2(K[t].z, qp.z, d); d = dot2(K[t].w, qp.w, d);
      PIN(d);
      K[t] = ld_nt(rv, vo + t * 1024);
      SCHED();
      sc[t] = row_sum(d);
    }
    float pm = m;
#pragma unroll
    for (int t = 0; t < TPC; t += 2) {
      const unsigned r1 = cvtpk_a(sc[t], sc[t + 1]);
      const unsigned r2 = cvtpk_a(lo16(r1) * SCALE, hi16(r1) * SCALE);
      sc[t] = lo16(r2); sc[t + 1] = hi16(r2);
      pm = fmx3(pm, sc[t], sc[t + 1]);
    }
    const float alpha = __builtin_amdgcn_exp2f((m - pm) * LOG2E);
    m = pm; l *= alpha;
    const float mo = -pm * LOG2E;
#pragma unroll
    for (int k = 0; k < 8; ++k) acc[k] *= alpha;
#pragma unroll
    for (int t = 0; t < TPC; ++t) { sc[t] = __builtin_amdgcn_exp2f(fmaf(sc[t], LOG2E, mo)); l += sc[t]; }
    vo = ((vo + 32 * CBYTES) & 0x1fffff) | (left > 0 ? 0 : OOB);
#pragma unroll
    for (int t = 0; t < TPC; ++t) {
      const float p = sc[t];
      acc[0] = fmaf(p, lo16(K[t].x), acc[0]); acc[1] = fmaf(p, hi16(K[t].x), acc[1]);
      acc[2] = fmaf(p, lo16(K[t].y), acc[2]); acc[3] = fmaf(p, hi16(K[t].y), acc[3]);
      acc[4] = fmaf(p, lo16(K[t].z), acc[4]); acc[5] = fmaf(p, hi16(K[t].z), acc[5]);
      acc[6] = fmaf(p, lo16(K[t].w), acc[6]); acc[7] = fmaf(p, hi16(K[t].w), acc[7]);
      asm volatile("" : "+v"(acc[0]), "+v"(acc[1]), "+v"(acc[2]), "+v"(acc[3]), "+v"(acc[4]), "+v"(acc[5]), "+v"(acc[6]), "+v"(acc[7]));
      if (t == 0) {
        asm volatile("" : "+s"(now));
        now -= t0;
        const int due = left > 0 ? send - ((left * SLOPE) >> 8) : 0;
        [[maybe_unused]] int dlt = 30;
        if ((int)now < due) {
          now = (unsigned)__builtin_amdgcn_s_memrealtime() - t0;
          dlt = (int)now - due + TGT;
          while ((int)now < due) { __builtin_amdgcn_s_sleep(4); now = (unsigned)__builtin_amdgcn_s_memrealtime() - t0; }
        }
        if (TGT && left <= NCH - 32) { send += dlt > 30 ? 30 : dlt < -15 ? -15 : dlt; send = send < send0 ? send0 : send; }
      }
      K[t] = ld_nt(rk, vo + t * 1024);
      SCHED();
    }
  }
  auto keep_ring = [&]() {
    SCHED();
#pragma unroll
    for (int t = 0; t < TPC; ++t) asm volatile("" :: "v"(K[t]));
  };
  {
    const float M = x32_max(x16_max(m));
    const float w = __builtin_amdgcn_exp2f((m - M) * LOG2E);
    float a[9];
#pragma unroll
    for (int k = 0; k < 8; ++k) a[k] = acc[k] * w;
    a[8] = l * w;
    SCHED();
    const float t0_ = pair32(a[0], a[4]), t1 = pair32(a[1], a[5]), t2 = pair32(a[2], a[6]), t3 = pair32(a[3], a[7]), t4 = pair32(a[8], a[8]);
    SCHED();
    const float u0 = pair16(t0_, t2), u1 = pair16(t1, t3), ls = pair16(t4, t4);
    *(LAS f32x2*)(L + a_mw) = f32x2{u0, u1};
    *(LAS f32x2*)(L + L_ML + wave * 8) = f32x2{M, ls};
  }
  BAR();
  if (wave == 0) {
    const f32x2 ml = *(LAS f32x2*)(L + a_ml);
    f32x2 a[8];
#pragma unroll
    for (int w = 0; w < 8; ++w) a[w] = *(LAS f32x2*)(L + a_mr + w * 512);
    SCHED();
    const float M = max8(ml.x);
    const float e = __builtin_amdgcn_exp2f((ml.x - M) * LOG2E);
    float ls = e * ml.y;
    ls += dpp<XOR7>(ls); ls += dpp<XOR2>(ls); ls += dpp<XOR1>(ls);
    const float ew[8] = {dpp<0x150>(e), dpp<0x151>(e), dpp<0x152>(e), dpp<0x153>(e), dpp<0x154>(e), dpp<0x155>(e), dpp<0x156>(e), dpp<0x157>(e)};
    f32x2 o = {0.f, 0.f}, o2 = o;
#pragma unroll
    for (int w = 0; w < 8; w += 2) { o += ew[w] * a[w]; o2 += ew[w + 1] * a[w + 1]; }
    o += o2;
    const bool odd = lane & 1;
    st_sc1(rws, aoff, pack4(odd ? M : o.x, odd ? ls : o.y, dpp<XOR1>(o.x), dpp<XOR1>(o.y), km, tg));
    keep_ring();
  } else {
    keep_ring();
  }

  if (is_ffn) {
    static_assert(GL <= 54 && GLX <= 54, "waves 0, 1 read their fragments (first half of gate) in front of the late weights");
    if (wave >= 2) {
      gu_dma(72 - gl - 2, 72, 6);
      const unsigned char* db = (const unsigned char*)(wd + (size_t)fidx * 32 * D);
      for (int d = wave - 2; d < 32; d += 6) dma16(db + d * 1152 + lane * 16, L_D + d * PITCH);
      if (wave >= 4) { const int row = (wave - 4) * 8 + (lane >> 3); dma16(db + row * 1152 + 1024 + (((lane & 7) ^ (2 * ((row >> 1) & 3))) * 16), L_DT + (wave - 4) * 1024); }
      asm volatile("s_waitcnt vmcnt(0)" ::: "memory");
      lds_inc(F_LAND);
      while (flag(F_LAND) < 6) {}
      FENCE();
    }
    const int mt = wave >> 2, kh = (wave >> 1) & 1, tl = wave & 1;
    u32x4 bg[9], bd[6];
    {
      const int g = lane >> 4, q = (lane & 15) >> 2, p = lane & 3;
      const int r0 = 288 * kh + 8 * g + q;
      LAS unsigned char* a0 = L + (mt ? L_U : L_G) + r0 * 64 + ((tl ^ (g & 1)) * 32) + p * 8;
#pragma unroll
      for (int i = 0; i < 9; ++i) {
        const u32x2 u0 = trread(a0 + i * 2048), u1 = trread(a0 + i * 2048 + 256);
        bg[i] = u32x4{u0.x, u0.y, u1.x, u1.y};
      }
    }
    if (wave >= 2) {
#pragma unroll
      for (int i = 0; i < 5; ++i) bd[i] = bfrag576<false>(L, L_D, wave - 2 + 6 * i, 0, lane);
      bd[5] = wave < 4 ? bfrag576<false>(L, L_D, wave + 28, 0, lane) : bfrag576<true>(L, L_DT, wave - 4, 0, lane);
    }
    int o4 = (lane & 1) ? OOB : WS_OP + fidx * 128 + (lane & 14) * 8 + (wave - 2 + 6 * (lane >> 4)) * OTS;
    int o1 = (lane & 33) ? OOB : o4 + 24 * OTS;
    int a_xn = L_XN + kh * 576 + (lane & 1) * 1152 + (lane >> 4) * 16, a_gw = L_GU + wave * 128 + (lane & 15) * 8;
    int a_gr = L_GU + (lane & 15) * 8 + (lane >> 5) * 4 + (lane & 16) * 8;
    int a_aw = L_ACT + wave * 128 + lane * 2, a_ar = L_ACT + wave * 128 + (lane & 1) * 64 + (lane >> 4) * 16;
    PIN(o4); PIN(o1); PIN(a_xn); PIN(a_gw); PIN(a_gr); PIN(a_aw); PIN(a_ar);
    if (wave < 2) {
      int off[3]; u32x4 v[3];
      off[0] = WS_HP + j * HRS + (lane >> 1) * 128 + wave * 32 + (lane & 1) * 16; off[1] = off[0] + 32 * 128; off[2] = lane < 16 ? off[0] + 64 * 128 : OOB;
      int a_x0 = L_XN + wave * 1152 + lane * 8, a_x2 = lane < 16 ? a_x0 + 1024 : L_TRASH + lane * 8;
      PIN(a_x0); PIN(a_x2);
      while (flag(F_LAND) < 1) __builtin_amdgcn_s_sleep(1);
      {
        const int o_[1] = {WS_HP + j * HRS + (fidx >> 1) * 128 + wave * 32 + (lane & 1) * 16}; u32x4 v_[1];
        gather<1, false>(rws, o_, v_, tg);
      }
      gather<3, false>(rws, off, v, tg, lane < 16);
      const unsigned hp[6] = {v[0].x, v[0].z, v[1].x, v[1].z, v[2].x, v[2].z};
      float s1 = 0.f, s2 = 0.f, s1b = 0.f, s2b = 0.f;
#pragma unroll
      for (int i = 0; i < 6; i += 2) {
        s1 = dot2(hp[i], 0x3f803f80u, s1); s2 = dot2(hp[i], hp[i], s2);
        s1b = dot2(hp[i + 1], 0x3f803f80u, s1b); s2b = dot2(hp[i + 1], hp[i + 1], s2b);
      }
      float st = pair32(s1 + s1b, s2 + s2b);
      st = row_sum(pair16(st, st)) * (1.0f / D);
      const float mean = __uint_as_float(__builtin_amdgcn_readlane(__float_as_uint(st), 0)), msq = __uint_as_float(__builtin_amdgcn_readlane(__float_as_uint(st), 32));
      const unsigned mv = cvtpk_a(mean, fmx(fmaf(-mean, mean, msq), 0.f));
      const float nmb = -lo16(mv), den = rbf(__builtin_amdgcn_sqrtf(rbf(hi16(mv) + 1e-5f))), rden = __builtin_amdgcn_rcpf(den);
      unsigned e[6], o[6];
#pragma unroll
      for (int i = 0; i < 6; ++i) { const f32x2 t = f32x2{lo16(hp[i]), hi16(hp[i])} + nmb; e[i] = cvtpk_a(t.x, t.y); }
#pragma unroll
      for (int i = 0; i < 6; ++i) { const f32x2 t = f32x2{lo16(e[i]), hi16(e[i])} * rden; o[i] = cvtpk(t.x, t.y); }
      *(LAS u32x2*)(L + a_x0) = u32x2{o[0], o[1]}; *(LAS u32x2*)(L + a_x0 + 512) = u32x2{o[2], o[3]};
      *(LAS u32x2*)(L + a_x2) = u32x2{o[4], o[5]};
    }
    BAR();
    {
      u32x4 af[9];
#pragma unroll
      for (int i = 0; i < 9; ++i) af[i] = *(LAS u32x4*)(L + a_xn + i * 64);
      SCHED();
      f32x4 c = {0.f, 0.f, 0.f, 0.f}, c2 = c;
#pragma unroll
      for (int i = 0; i < 8; i += 2) { c = mfma(af[i], bg[i], c); c2 = mfma(af[i + 1], bg[i + 1], c2); }
      c = mfma(af[8], bg[8], c);
      *(LAS f32x2*)(L + a_gw) = f32x2{c[0] + c2[0], c[1] + c2[1]};
    }
    BAR();
    if (wave >= 2) {
      LAS float* gu = (LAS float*)(L + a_gr);
      const float g0 = gu[0], g1 = gu[64], u0 = gu[128], u1 = gu[192];
      SCHED();
      const unsigned gb = cvtpk_a(g0 + g1, u0 + u1);
      const float g = lo16(gb), u = hi16(gb);
      constexpr float K0 = -2.0f * LOG2E * 0.7978845608028654f;
      const float dn = 1.0f + __builtin_amdgcn_exp2f(g * fmaf(g * g, K0 * 0.044715f, K0));
      const float ge = rbf(g * __builtin_amdgcn_rcpf(dn));
      *(LAS u16*)(L + a_aw) = (u16)cvtpk(ge * u, 0.f);
      LGKM0();
      const u32x4 a = *(LAS u32x4*)(L + a_ar);
      const f32x4 z = {0.f, 0.f, 0.f, 0.f};
      f32x4 c[6];
#pragma unroll
      for (int i = 0; i < 6; ++i) c[i] = mfma(a, bd[i], z);
      publish_tiles<6>(rws, c, o4, o1, km, lane, tg);
    }
    return;
  }
  if (is_wo) {
    u32x4 bw[5][2];
#pragma unroll
    for (int ks = 0; ks < 2; ++ks) {
#pragma unroll
      for (int i = 0; i < 4; ++i) bw[i][ks] = bfrag576<false>(L, L_WO, wave + 8 * i, ks, lane);
      bw[4][ks] = bfrag576<true>(L, L_WOT, wave & 3, ks, lane);
    }
    const int yb = WS_YP + (b * 32 + hw) * 128 + (lane & 14) * 8;
    int o4 = (lane & 1) ? OOB : yb + (wave + 8 * (lane >> 4)) * YTS;
    int o1 = ((lane & 49) || wave >= 4) ? OOB : yb + (32 + wave) * YTS;
    int a_or = L_O16 + (lane & 1) * 128 + (lane >> 4) * 16;
    PIN(o4); PIN(o1); PIN(a_or);
    if (wave == 0) {
      int off[4]; u32x4 v[4];
      const int base = WS_AP + ((hw * 8 + (lane >> 5) * 4 + ((lane >> 4) & 1)) * 2 + b) * 384;
      off[0] = base + (lane & 15) * 16; off[1] = off[0] + 4 * 384; off[2] = base + 256; off[3] = base + 256 + 4 * 384;
      int a_ow = L_O16 + (lane >> 5) * 128 + (lane & 15) * 8 + ((lane >> 4) & 1) * 4;
      PIN(a_ow);
      gather<4, true>(rws, off, v, tg);
      const float m0 = __uint_as_float(v[2].x), m1 = __uint_as_float(v[3].x);
      const float M = x16_max(fmx(m0, m1));
      const float e0 = __builtin_amdgcn_exp2f((m0 - M) * LOG2E), e1 = __builtin_amdgcn_exp2f((m1 - M) * LOG2E);
      const float lp = fmaf(e0, __uint_as_float(v[2].y), e1 * __uint_as_float(v[3].y));
      const float rl = __builtin_amdgcn_rcpf(pair16(lp, lp));
      const f32x4 o = e0 * __builtin_bit_cast(f32x4, v[0]) + e1 * __builtin_bit_cast(f32x4, v[1]);
      *(LAS unsigned*)(L + a_ow) = cvtpk(pair16(o[0], o[2]) * rl, pair16(o[1], o[3]) * rl);
    }
    BAR();
    const u32x4 a0 = *(LAS u32x4*)(L + a_or), a1 = *(LAS u32x4*)(L + a_or + 64);
    const f32x4 z = {0.f, 0.f, 0.f, 0.f};
    f32x4 c[5];
#pragma unroll
    for (int i = 0; i < 5; ++i) c[i] = mfma(a0, bw[i][0], z);
#pragma unroll
    for (int i = 0; i < 5; ++i) c[i] = mfma(a1, bw[i][1], c[i]);
    publish_tiles<5>(rws, c, o4, o1, km, lane, tg);
  }
  if (rho >= 0 && wave == 0) {
    const int uo = (lane >> 2) * 128 + (rho & 1) * 64 + (lane & 3) * 16;
    float xf = lo16(xr), hf;
    int oh = (lane & 1) ? OOB : WS_HP + (((lane >> 2) & 3) + (lane >> 5) * 4) * HRS + rho * 128 + (lane & 16) * 2 + (lane & 2) * 8;
    int oo = (lane & 12) ? OOB : rcol * 2, oe = (rho == 0 && lane == 0) ? WS_EPOCH : OOB;
    PIN(xf); PIN(oh); PIN(oo); PIN(oe);
    {
      int off[4]; u32x4 v[4];
#pragma unroll
      for (int i = 0; i < 4; ++i) off[i] = WS_YP + (rho >> 1) * YTS + i * 2048 + uo;
      gather<4, true>(rws, off, v, tg);
      const unsigned hb = cvtpk(xf + rbf(reduce_units<4>(v)), 0.f);
      hf = lo16(hb);
      auto sw = __builtin_amdgcn_permlane32_swap(hb, hb, false, false);
      const unsigned pk = sw[0] | (sw[1] << 16);
      st_sc1(rws, oh, u32x4{pk, tg.a, dppu<XOR1>(pk), tg.b});
    }
    {
      int off[9]; u32x4 v[9];
#pragma unroll
      for (int i = 0; i < 9; ++i) off[i] = WS_OP + (rho >> 1) * OTS + i * 2048 + uo;
      const rsrc_t ro = mkrsrc(out, 2 * D * 2);
      {
        const int o_[1] = {off[4] - (lane >> 2) * 128}; u32x4 v_[1];
        gather<1, true>(rws, o_, v_, tg);
      }
      gather<9, true>(rws, off, v, tg);
      const unsigned r = cvtpk(hf + rbf(reduce_units<9>(v)), 0.f);
      __builtin_amdgcn_raw_buffer_store_b16((u16)r, ro, oo, 0, 0);
      __builtin_amdgcn_raw_buffer_store_b64(u32x2{tg.a, tg.b}, rws, oe, 0, 16);
    }
  }
}

extern "C" void kernel_launch(void* const* d_in, const int* in_sizes, int n_in, void* d_out, int out_size, void* d_ws, size_t ws_size, hipStream_t stream) {
  (void)hipFuncSetAttribute((const void*)fused_block, hipFuncAttributeMaxDynamicSharedMemorySize, L_END);
  unsigned char* ws = (unsigned char*)d_ws + ((ws_size - ((size_t)4 << 20)) & ~(size_t)0x1fffff);
  fused_block<<<NWG, NTHR, L_END, stream>>>((const u16*)d_in[0], (const u16*)d_in[1], (const u16*)d_in[2], (const u16*)d_in[3], (const u16*)d_in[4],
                                           (const u16*)d_in[6], (const u16*)d_in[7], (const u16*)d_in[8], (u16*)d_out, ws);
}
```

```cpp
#include <hip/hip_runtime.h>
#include <stdint.h>

typedef unsigned short u16;
typedef unsigned u32x4 __attribute__((ext_vector_type(4)));
typedef unsigned u32x2 __attribute__((ext_vector_type(2)));
typedef float f32x4 __attribute__((ext_vector_type(4)));
typedef float f32x2 __attribute__((ext_vector_type(2)));
typedef short s16x4 __attribute__((ext_vector_type(4)));
typedef __bf16 bf16x2 __attribute__((ext_vector_type(2)));
typedef __bf16 bf16x8 __attribute__((ext_vector_type(8)));
typedef __amdgpu_buffer_rsrc_t rsrc_t;
#define LAS __attribute__((address_space(3)))

constexpr int D = 576, H = 32, SK = 8192, DH = 128, DF = 4608;
constexpr int NWG = 256, NTHR = 512;
constexpr int TPC = 4, CBYTES = 4096, NCH = 128;
constexpr unsigned kSpin = 1u << 17;
constexpr int OOB = 0x40000000;
constexpr int QDELAY = 25;
constexpr int GL = 38, GLX = 22;
constexpr int TEND = 3650, SLOPE = 134 * 256 / 5;
constexpr int STAG = 60;

constexpr int HRS = 73 * 128, YTS = 65 * 128, OTS = 145 * 128;
constexpr int WS_EPOCH = 0;
constexpr int WS_HP = 4096;
constexpr int WS_AP = 81920;
constexpr int WS_QP = WS_AP + 256 * 2 * 384;
constexpr int WS_YP = WS_QP + 32 * 8 * 2 * 1024;
constexpr int WS_OP = WS_YP + 36 * YTS;
constexpr int WS_END = WS_OP + 36 * OTS;

constexpr int PITCH = 1056;
constexpr int L_WO = 0, L_WOT = 64 * PITCH;
constexpr int L_G = 0, L_U = 36864, L_D = 73728, L_DT = L_D + 32 * PITCH;
constexpr int L_RED = L_DT + 32 * 128;
constexpr int L_Q16 = L_RED + 4096;
constexpr int L_O16 = L_Q16 + 256;
constexpr int L_XN = L_O16 + 256;
constexpr int L_GU = L_XN + 2304;
constexpr int L_ACT = L_GU + 1024;
constexpr int L_ML = L_ACT + 1024;
constexpr int L_TRASH = L_ML + 64;
constexpr int L_CTR = L_TRASH + 512;
constexpr int L_END = L_CTR + 64;
static_assert(L_WOT + 64 * 128 <= L_RED && L_END > 80 * 1024, "one workgroup per CU");
enum { F_QCNT, F_QRDY, F_POLL, F_T0, F_LAND };

__device__ __forceinline__ float lo16(unsigned u) { return __uint_as_float(u << 16); }
__device__ __forceinline__ float hi16(unsigned u) { return __uint_as_float(u & 0xffff0000u); }
__device__ __forceinline__ unsigned cvtpk(float lo, float hi) {
  bf16x2 r; r.x = (__bf16)lo; r.y = (__bf16)hi; return __builtin_bit_cast(unsigned, r);
}
__device__ __forceinline__ unsigned cvtpk_a(float lo, float hi) {
  unsigned r; asm("v_cvt_pk_bf16_f32 %0, %1, %2" : "=v"(r) : "v"(lo), "v"(hi)); return r;
}
__device__ __forceinline__ float rbf(float f) { return lo16(cvtpk(f, f)); }
__device__ __forceinline__ float fmx(float a, float b) {
  float r; asm("v_max_f32 %0, %1, %2" : "=v"(r) : "v"(a), "v"(b)); return r;
}
__device__ __forceinline__ float fmx3(float a, float b, float c) {
  float r; asm("v_max3_f32 %0, %1, %2, %3" : "=v"(r) : "v"(a), "v"(b), "v"(c)); return r;
}
__device__ __forceinline__ float max8(float x) {
  float r;
  asm("s_nop 1\n\tv_max_f32_dpp %0, %1, %1 row_half_mirror row_mask:0xf bank_mask:0xf\n\ts_nop 1\n\t"
      "v_max_f32_dpp %0, %0, %0 quad_perm:[2,3,0,1] row_mask:0xf bank_mask:0xf\n\ts_nop 1\n\t"
      "v_max_f32_dpp %0, %0, %0 quad_perm:[1,0,3,2] row_mask:0xf bank_mask:0xf" : "=&v"(r) : "v"(x));
  return r;
}
__device__ __forceinline__ float dot2(unsigned k, unsigned q, float acc) {
  return __builtin_amdgcn_fdot2_f32_bf16(__builtin_bit_cast(bf16x2, k), __builtin_bit_cast(bf16x2, q), acc, false);
}
template <int CTRL> __device__ __forceinline__ float dpp(float x) {
  return __builtin_bit_cast(float, __builtin_amdgcn_mov_dpp(__builtin_bit_cast(int, x), CTRL, 0xf, 0xf, true));
}
template <int CTRL> __device__ __forceinline__ unsigned dppu(unsigned x) {
  return (unsigned)__builtin_amdgcn_mov_dpp((int)x, CTRL, 0xf, 0xf, true);
}
constexpr int XOR1 = 0xB1, XOR2 = 0x4E, XOR7 = 0x141, XOR8 = 0x128, ROR4 = 0x124;
__device__ __forceinline__ float pair32(float a, float b) {
  auto s = __builtin_amdgcn_permlane32_swap(__float_as_uint(a), __float_as_uint(b), false, false);
  return __uint_as_float(s[0]) + __uint_as_float(s[1]);
}
__device__ __forceinline__ float pair16(float a, float b) {
  auto s = __builtin_amdgcn_permlane16_swap(__float_as_uint(a), __float_as_uint(b), false, false);
  return __uint_as_float(s[0]) + __uint_as_float(s[1]);
}
__device__ __forceinline__ float x16_max(float x) {
  auto s = __builtin_amdgcn_permlane16_swap(__float_as_uint(x), __float_as_uint(x), false, false);
  return fmx(__uint_as_float(s[0]), __uint_as_float(s[1]));
}
__device__ __forceinline__ float x32_max(float x) {
  auto s = __builtin_amdgcn_permlane32_swap(__float_as_uint(x), __float_as_uint(x), false, false);
  return fmx(__uint_as_float(s[0]), __uint_as_float(s[1]));
}
__device__ __forceinline__ float row_sum(float d) { d += dpp<XOR8>(d); d += dpp<XOR7>(d); d += dpp<XOR2>(d); d += dpp<XOR1>(d); return d; }
__device__ __forceinline__ float fdiv(float a, float b, float rb) {
  const float q = a * rb; return fmaf(fmaf(-q, b, a), rb, q);
}
#define BAR() asm volatile("s_waitcnt lgkmcnt(0)\n\ts_barrier" ::: "memory")
#define LGKM0() asm volatile("s_waitcnt lgkmcnt(0)" ::: "memory")
#define FENCE() asm volatile("" ::: "memory")
#define PIN(x) asm volatile("" : "+v"(x))
#define SCHED() __builtin_amdgcn_sched_barrier(0)
__device__ __forceinline__ void dma16(const void* gsrc, unsigned lds_dst) {
  unsigned keep;
  asm volatile("s_mov_b32 %0, m0\n\ts_mov_b32 m0, %2\n\ts_nop 0\n\tglobal_load_lds_dwordx4 %1, off nt\n\ts_mov_b32 m0, %0" : "=&s"(keep) : "v"(gsrc), "s"(lds_dst) : "memory");
}
__device__ __forceinline__ rsrc_t mkrsrc(const void* p, int bytes) { return __builtin_amdgcn_make_buffer_rsrc((void*)p, 0, bytes, 0x00020000); }
__device__ __forceinline__ u32x4 ld_sc1(rsrc_t r, int off) { return __builtin_amdgcn_raw_buffer_load_b128(r, off, 0, 16); }
__device__ __forceinline__ void st_sc1(rsrc_t r, int off, u32x4 v) { __builtin_amdgcn_raw_buffer_store_b128(v, r, off, 0, 16); }
__device__ __forceinline__ u32x4 ld_nt(rsrc_t r, int off) { return __builtin_amdgcn_raw_buffer_load_b128(r, off, 0, 2); }
__device__ __forceinline__ u32x2 trread(LAS unsigned char* p) {
  return __builtin_bit_cast(u32x2, __builtin_amdgcn_ds_read_tr16_b64_v4i16((LAS s16x4*)p));
}
__device__ __forceinline__ f32x4 mfma(u32x4 a, u32x4 b, f32x4 c) {
  return __builtin_amdgcn_mfma_f32_16x16x32_bf16(__builtin_bit_cast(bf16x8, a), __builtin_bit_cast(bf16x8, b), c, 0, 0, 0);
}

struct Tags { unsigned a, b, p0, p1, p2, p3; };
__device__ __forceinline__ u32x4 pack4(float v0, float v1, float v2, float v3, unsigned km, const Tags& t) {
  return u32x4{(__float_as_uint(v0) & km) | t.p0, (__float_as_uint(v1) & km) | t.p1, (__float_as_uint(v2) & km) | t.p2, (__float_as_uint(v3) & km) | t.p3};
}
__device__ __forceinline__ unsigned orxor(unsigned acc, unsigned x, unsigned t) { return __builtin_amdgcn_bitop3_b32(acc, x, t, 0xF6); }
template <int N, bool PACKED>
__device__ __forceinline__ void gather(rsrc_t rs, const int (&off)[N], u32x4 (&v)[N], const Tags& t, bool last = true) {
  for (unsigned spins = 0; spins < kSpin; ++spins) {
    FENCE();
#pragma unroll
    for (int i = 0; i < N; ++i) v[i] = ld_sc1(rs, off[i]);
    unsigned bad = 0;
#pragma unroll
    for (int i = 0; i < N; ++i) {
      if (i == N - 1 && N == 3) break;
      if (PACKED) { v[i].x ^= t.p0; v[i].y ^= t.p1; v[i].z ^= t.p2; v[i].w ^= t.p3; bad |= (v[i].x | v[i].y) | (v[i].z | v[i].w); }
      else bad = orxor(orxor(bad, v[i].y, t.a), v[i].w, t.b);
    }
    if (N == 3) bad |= last ? (v[N - 1].y ^ t.a) | (v[N - 1].w ^ t.b) : 0u;
    if (__builtin_amdgcn_ballot_w64((PACKED ? bad << 20 : bad) != 0u) == 0ull) break;
  }
}
template <bool TAIL>
__device__ __forceinline__ u32x4 bfrag576(LAS unsigned char* L, int base, int T, int ks, int lane) {
  const int g = lane >> 4, q = (lane & 15) >> 2, p = lane & 3;
  const int r0 = 32 * ks + 8 * g + q;
  u32x2 x, y;
  if (!TAIL) { LAS unsigned char* a = L + base + r0 * PITCH + T * 32 + p * 8; x = trread(a); y = trread(a + 4 * PITCH); }
  else { x = trread(L + base + r0 * 128 + ((T ^ ((r0 >> 1) & 3)) * 32) + p * 8); y = trread(L + base + (r0 + 4) * 128 + ((T ^ (((r0 + 4) >> 1) & 3)) * 32) + p * 8); }
  return u32x4{x.x, x.y, y.x, y.y};
}
template <int NT>
__device__ __forceinline__ void publish_tiles(rsrc_t rws, const f32x4 (&c)[NT], int o4, int o1, unsigned km, int lane, const Tags& t) {
  const bool g1 = lane & 16, g2 = lane & 32;
  const float a0 = g1 ? c[1][0] : c[0][0], b0 = g1 ? c[3][0] : c[2][0], a1 = g1 ? c[1][1] : c[0][1], b1 = g1 ? c[3][1] : c[2][1];
  const float s0 = g2 ? b0 : a0, s1 = g2 ? b1 : a1;
  st_sc1(rws, o4, pack4(s0, s1, dpp<XOR1>(s0), dpp<XOR1>(s1), km, t));
  const float r0 = g1 ? c[NT - 1][0] : c[4][0], r1 = g1 ? c[NT - 1][1] : c[4][1];
  st_sc1(rws, o1, pack4(r0, r1, dpp<XOR1>(r0), dpp<XOR1>(r1), km, t));
}
template <int N>
__device__ __forceinline__ float reduce_units(const u32x4 (&v)[N]) {
  f32x4 s = __builtin_bit_cast(f32x4, v[0]), s2 = __builtin_bit_cast(f32x4, v[1]);
#pragma unroll
  for (int i = 2; i < N; ++i) { if (i & 1) s2 += __builtin_bit_cast(f32x4, v[i]); else s += __builtin_bit_cast(f32x4, v[i]); }
  s += s2;
  float y = pair16(pair32(s[0], s[2]), pair32(s[1], s[3]));
  y += dpp<XOR8>(y);
  return y + dpp<ROR4>(y);
}

__global__ void __launch_bounds__(NTHR, 2)
fused_block(const u16* __restrict__ x, const u16* __restrict__ kc, const u16* __restrict__ vc, const u16* __restrict__ wq, const u16* __restrict__ wo,
            const u16* __restrict__ wg, const u16* __restrict__ wu, const u16* __restrict__ wd, u16* __restrict__ out, unsigned char* __restrict__ ws) {
  extern __shared__ __attribute__((aligned(16))) unsigned char smem[];
  LAS unsigned char* L = (LAS unsigned char*)smem;
  const int tid = threadIdx.x, lane = tid & 63, wave = __builtin_amdgcn_readfirstlane(tid >> 6);
  const int blk = blockIdx.x, h = blk >> 3, j = blk & 7, b = j >> 2, s = j & 3;
  asm volatile("" :: "s"(x), "s"(kc), "s"(vc), "s"(wq), "s"(ws));
  const rsrc_t rws = mkrsrc(ws, WS_END);
  u32x2 ep;
  Tags tg;
  auto tags = [&]() {
    PIN(ep);
    tg.a = __builtin_amdgcn_readfirstlane(ep.x) + 0x9E3779B1u; tg.b = __builtin_amdgcn_readfirstlane(ep.y) + 0x85EBCA77u;
    tg.p0 = tg.a & 0xfffu; tg.p1 = (tg.a >> 12) & 0xfffu; tg.p2 = tg.b & 0xfffu; tg.p3 = (tg.b >> 12) & 0xfffu;
  };
  volatile LAS int* flags = (volatile LAS int*)(L + L_CTR);
  auto flag = [&](int i) { return __builtin_amdgcn_readfirstlane(flags[i]); };
  auto lds_inc = [&](int i) { if (lane == 0) __hip_atomic_fetch_add((LAS int*)(L + L_CTR) + i, 1, __ATOMIC_RELAXED, __HIP_MEMORY_SCOPE_WORKGROUP); };
  u32x4 qv[8];
  const int qo = WS_QP + (h * 16 + b) * 1024;
  if (wave < 4) {
    ep = __builtin_amdgcn_raw_buffer_load_b64(rws, WS_EPOCH, 0, 16);
    BAR();
  } else {
    __builtin_amdgcn_s_setprio(3);
    if (lane < 16) ((LAS int*)(L + L_CTR))[lane] = 0;
    const rsrc_t rx = mkrsrc(x, 2 * D * 2), rq = mkrsrc(wq + h * DH, D * H * DH * 2 - h * DH * 2);
    const u32x4 xa = __builtin_amdgcn_raw_buffer_load_b128(rx, lane * 36, 0, 0);
    const u32x4 xb = __builtin_amdgcn_raw_buffer_load_b128(rx, lane * 36 + 16, 0, 0);
    const unsigned xc = __builtin_amdgcn_raw_buffer_load_b32(rx, lane * 36 + 32, 0, 0);
    const unsigned xq = __builtin_amdgcn_raw_buffer_load_b32(rx, (lane >> 5) * 1152 + ((lane & 15) < 8 ? lane & 15 : 8) * 4, (36 * j + 9 * (wave - 4)) * 4, 0);
    const int r0 = (72 * j + 18 * (wave - 4)) * (H * DH * 2);
    unsigned wv[18];
#pragma unroll
    for (int i = 0; i < 18; ++i) wv[i] = __builtin_amdgcn_raw_buffer_load_b32(rq, lane * 4, r0 + i * (H * DH * 2), 2);
    ep = __builtin_amdgcn_raw_buffer_load_b64(rws, WS_EPOCH, 0, 16);
    SCHED();
    BAR();
    if (wave == 4) flags[F_T0] = (int)(unsigned)__builtin_amdgcn_s_memrealtime();
    SCHED();
    LAS f32x2* red = (LAS f32x2*)(L + L_RED);
    const unsigned xd[9] = {xa.x, xa.y, xa.z, xa.w, xb.x, xb.y, xb.z, xb.w, xc};
    float s1 = 0.f, s2 = 0.f;
#pragma unroll
    for (int i = 0; i < 9; ++i) { s1 = dot2(xd[i], 0x3f803f80u, s1); s2 = dot2(xd[i], xd[i], s2); }
    const float st = row_sum(pair16(s1, s2)) * (1.0f / D);
    auto sw = __builtin_amdgcn_permlane16_swap(__float_as_uint(st), __float_as_uint(st), false, false);
    const float mean = __uint_as_float(sw[0]), msq = __uint_as_float(sw[1]);
    const unsigned mv = cvtpk_a(mean, fmx(fmaf(-mean, mean, msq), 0.f));
    const float den = rbf(__builtin_amdgcn_sqrtf(rbf(hi16(mv) + 1e-5f))), rden = __builtin_amdgcn_rcpf(den);
    const unsigned e = cvtpk_a(lo16(xq) - lo16(mv), hi16(xq) - lo16(mv));
    const unsigned xn = cvtpk_a(fdiv(lo16(e), den, rden), fdiv(hi16(e), den, rden));
    float a00 = 0.f, a01 = 0.f, a10 = 0.f, a11 = 0.f;
#pragma unroll
    for (int i = 0; i < 9; ++i) {
      const unsigned wl = __builtin_amdgcn_perm(wv[2 * i + 1], wv[2 * i], 0x05040100u), wh = __builtin_amdgcn_perm(wv[2 * i + 1], wv[2 * i], 0x07060302u);
      const unsigned x0 = __builtin_amdgcn_readlane(xn, i), x1 = __builtin_amdgcn_readlane(xn, 32 + i);
      a00 = dot2(wl, x0, a00); a01 = dot2(wh, x0, a01); a10 = dot2(wl, x1, a10); a11 = dot2(wh, x1, a11);
    }
    red[((wave - 4) * 2 + 0) * 64 + lane] = f32x2{a00, a01};
    red[((wave - 4) * 2 + 1) * 64 + lane] = f32x2{a10, a11};
    tags();
    LGKM0();
    lds_inc(F_QCNT);
    if (wave < 6) {
      while (flag(F_QCNT) < 4) {}
      FENCE();
      f32x2 t = red[(wave - 4) * 64 + lane];
#pragma unroll
      for (int w = 1; w < 4; ++w) t += red[(w * 2 + wave - 4) * 64 + lane];
      st_sc1(rws, WS_QP + ((h * 8 + j) * 2 + wave - 4) * 1024 + lane * 16, u32x4{__float_as_uint(t.x), tg.a, __float_as_uint(t.y), tg.b});
    }
    if (wave == 4) {
      __builtin_amdgcn_s_sleep(QDELAY);
      FENCE();
#pragma unroll
      for (int p = 0; p < 8; ++p) qv[p] = __builtin_amdgcn_raw_buffer_load_b128(rws, lane * 16, qo + p * 2048, 16);
      FENCE();
      flags[F_POLL] = 1;
    } else {
      __builtin_amdgcn_s_setprio(0);
      if (wave == 7) { while (flag(F_POLL) == 0) __builtin_amdgcn_s_sleep(1); }
    }
  }
  SCHED();
  const size_t kvo = (size_t)(b * 32 + h) * SK * DH;
  const rsrc_t rk = mkrsrc(kc + kvo, SK * DH * 2), rv = mkrsrc(vc + kvo, SK * DH * 2);
  int vo = (s * 8 + wave) * CBYTES + lane * 16 + ((h & 15) << 17);
  u32x4 K[TPC];
#pragma unroll
  for (int t = 0; t < TPC; ++t) { K[t] = ld_nt(rk, vo + t * 1024); SCHED(); }
  if (wave == 4) {
    unsigned bad = 0;
#pragma unroll
    for (int p = 0; p < 8; ++p) bad = orxor(orxor(bad, qv[p].y, tg.a), qv[p].w, tg.b);
    if (__builtin_amdgcn_ballot_w64(bad != 0u) != 0ull) {
      int qoff[8];
#pragma unroll
      for (int p = 0; p < 8; ++p) qoff[p] = qo + p * 2048 + lane * 16;
      gather<8, false>(rws, qoff, qv, tg);
    }
    float q0 = 0.f, q1 = 0.f;
#pragma unroll
    for (int p = 0; p < 8; ++p) { q0 += __uint_as_float(qv[p].x); q1 += __uint_as_float(qv[p].z); }
    ((LAS unsigned*)(L + L_Q16))[lane] = cvtpk(q0, q1);
    LGKM0();
    flags[F_QRDY] = 1;
    __builtin_amdgcn_s_setprio(0);
  }
  SCHED();
  const int rr = (s - h) & 3, hq = (h >> 2) & 3;
  const bool is_wo = (rr == 0 && hq != 3) || (rr == 3 && hq == 2), is_ffn = rr == 1 || rr == 2 || (rr == 3 && hq == 3);
  const int hw = rr == 0 ? h : h + 4;
  const int fidx = rr == 1 ? 2 * (b * 32 + h) : rr == 2 ? 2 * (b * 32 + ((h + 1) & 31)) + 1 : 128 + 2 * (b * 4 + (h & 3)) + (h >> 4);
  const int gl = (((int)((size_t)wg >> 7) + (fidx >> 1)) & 7) == 3 ? GLX : GL;
  auto gu_dma = [&](int p0, int p1, int n) {
    const int sw = ((lane & 3) ^ (2 * ((lane >> 5) & 1))) * 8, r = lane >> 2;
    for (int p = p0 + wave; p < p1; p += n) {
      const int q = p >= 36 ? p - 36 : p;
      dma16((p >= 36 ? wu : wg) + (size_t)(q * 16 + r) * DF + fidx * 32 + sw, L_G + p * 1024);
    }
  };
  if (wave < 4) {
    if (is_wo) {
      const unsigned char* wb = (const unsigned char*)(wo + (size_t)(hw * DH + b * 64) * D);
#pragma unroll 4
      for (int i = 0; i < 16; ++i) dma16(wb + (wave * 16 + i) * 1152 + lane * 16, L_WO + (wave * 16 + i) * PITCH);
#pragma unroll
      for (int i = 0; i < 2; ++i) {
        const int row = wave * 16 + i * 8 + (lane >> 3);
        dma16(wb + row * 1152 + 1024 + (((lane & 7) ^ (2 * ((row >> 1) & 3))) * 16), L_WOT + (wave * 2 + i) * 1024);
      }
    } else if (is_ffn) {
      gu_dma(0, 72 - gl, 4);
    }
  }
  SCHED();
  tags();
  const int rho = (rr == 3 && hq < 2) ? b * 16 + (h >> 4) * 8 + (h & 3) * 2 + hq : (rr == 0 && hq == 3) ? 32 + b * 8 + (h & 3) * 2 + (h >> 4)
                : (rr == 0 && hq < 2 && (b == 0 || h < 8)) ? 48 + b * 16 + (h >> 4) * 8 + (h & 3) * 2 + hq : -1;
  const int rcol = (lane & 16) * (D / 16) + 8 * rho + 2 * (lane & 3) + (lane >> 5);
  const unsigned xr = __builtin_amdgcn_raw_buffer_load_b16(mkrsrc(x, 2 * D * 2), (rho >= 0 && wave == 0) ? rcol * 2 : OOB, 0, 0);
  while (flag(F_QRDY) == 0) __builtin_amdgcn_s_sleep(1);
  FENCE();
  const u32x4 qp = *(LAS u32x4*)(L + L_Q16 + (lane & 15) * 16);

  constexpr float SCALE = 0.088388347648318440f, LOG2E = 1.4426950408889634f;
  float m = -3.0e38f, l = 0.f, acc[8];
#pragma unroll
  for (int k = 0; k < 8; ++k) acc[k] = 0.f;
  int aoff = (lane & 30) * 8;
  aoff = (lane & 1) ? OOB : aoff;
  aoff = (lane & 31) == 1 ? 256 : aoff;
  aoff += WS_AP + (blk * 2 + (lane >> 5)) * 384;
  int a_mw = L_RED + wave * 512 + (lane & 15) * 32 + (lane >> 4) * 8, a_mr = L_RED + lane * 8, a_ml = L_ML + (lane & 7) * 8;
  unsigned km = 0xfffff000u;
  PIN(aoff); PIN(a_mw); PIN(a_mr); PIN(a_ml); PIN(km);
  const unsigned t0 = (unsigned)flag(F_T0);
  const int send = TEND + ((((h * 13) & 31) * STAG) >> 5) - STAG / 2;
  for (int left = NCH - 8 - wave; left > -8; left -= 8) {
    unsigned now = (unsigned)__builtin_amdgcn_s_memrealtime();
    float sc[TPC];
#pragma unroll
    for (int t = 0; t < TPC; ++t) {
      float d = dot2(K[t].x, qp.x, 0.f); d = dot2(K[t].y, qp.y, d); d = dot2(K[t].z, qp.z, d); d = dot2(K[t].w, qp.w, d);
      PIN(d);
      K[t] = ld_nt(rv, vo + t * 1024);
      SCHED();
      sc[t] = row_sum(d);
    }
    float pm = m;
#pragma unroll
    for (int t = 0; t < TPC; t += 2) {
      const unsigned r1 = cvtpk_a(sc[t], sc[t + 1]);
      const unsigned r2 = cvtpk_a(lo16(r1) * SCALE, hi16(r1) * SCALE);
      sc[t] = lo16(r2); sc[t + 1] = hi16(r2);
      pm = fmx3(pm, sc[t], sc[t + 1]);
    }
    const float alpha = __builtin_amdgcn_exp2f((m - pm) * LOG2E);
    m = pm; l *= alpha;
    const float mo = -pm * LOG2E;
#pragma unroll
    for (int k = 0; k < 8; ++k) acc[k] *= alpha;
#pragma unroll
    for (int t = 0; t < TPC; ++t) { sc[t] = __builtin_amdgcn_exp2f(fmaf(sc[t], LOG2E, mo)); l += sc[t]; }
    vo = ((vo + 32 * CBYTES) & 0x1fffff) | (left > 0 ? 0 : OOB);
#pragma unroll
    for (int t = 0; t < TPC; ++t) {
      const float p = sc[t];
      acc[0] = fmaf(p, lo16(K[t].x), acc[0]); acc[1] = fmaf(p, hi16(K[t].x), acc[1]);
      acc[2] = fmaf(p, lo16(K[t].y), acc[2]); acc[3] = fmaf(p, hi16(K[t].y), acc[3]);
      acc[4] = fmaf(p, lo16(K[t].z), acc[4]); acc[5] = fmaf(p, hi16(K[t].z), acc[5]);
      acc[6] = fmaf(p, lo16(K[t].w), acc[6]); acc[7] = fmaf(p, hi16(K[t].w), acc[7]);
      asm volatile("" : "+v"(acc[0]), "+v"(acc[1]), "+v"(acc[2]), "+v"(acc[3]), "+v"(acc[4]), "+v"(acc[5]), "+v"(acc[6]), "+v"(acc[7]));
      if (t == 0) {
        asm volatile("" : "+s"(now));
        now -= t0;
        const int due = left > 0 ? send - (((left > 3 ? left : 3) * SLOPE) >> 8) : 0;
        if ((int)now < due) {
          now = (unsigned)__builtin_amdgcn_s_memrealtime() - t0;
          while ((int)now < due) { __builtin_amdgcn_s_sleep(4); now = (unsigned)__builtin_amdgcn_s_memrealtime() - t0; }
        }
      }
      K[t] = ld_nt(rk, vo + t * 1024);
      SCHED();
    }
  }
  auto keep_ring = [&]() {
    SCHED();
#pragma unroll
    for (int t = 0; t < TPC; ++t) asm volatile("" :: "v"(K[t]));
  };
  {
    const float M = x32_max(x16_max(m));
    const float w = __builtin_amdgcn_exp2f((m - M) * LOG2E);
    float a[9];
#pragma unroll
    for (int k = 0; k < 8; ++k) a[k] = acc[k] * w;
    a[8] = l * w;
    SCHED();
    const float t0_ = pair32(a[0], a[4]), t1 = pair32(a[1], a[5]), t2 = pair32(a[2], a[6]), t3 = pair32(a[3], a[7]), t4 = pair32(a[8], a[8]);
    SCHED();
    const float u0 = pair16(t0_, t2), u1 = pair16(t1, t3), ls = pair16(t4, t4);
    *(LAS f32x2*)(L + a_mw) = f32x2{u0, u1};
    *(LAS f32x2*)(L + L_ML + wave * 8) = f32x2{M, ls};
  }
  BAR();
  if (wave == 0) {
    const f32x2 ml = *(LAS f32x2*)(L + a_ml);
    f32x2 a[8];
#pragma unroll
    for (int w = 0; w < 8; ++w) a[w] = *(LAS f32x2*)(L + a_mr + w * 512);
    SCHED();
    const float M = max8(ml.x);
    const float e = __builtin_amdgcn_exp2f((ml.x - M) * LOG2E);
    float ls = e * ml.y;
    ls += dpp<XOR7>(ls); ls += dpp<XOR2>(ls); ls += dpp<XOR1>(ls);
    const float ew[8] = {dpp<0x150>(e), dpp<0x151>(e), dpp<0x152>(e), dpp<0x153>(e), dpp<0x154>(e), dpp<0x155>(e), dpp<0x156>(e), dpp<0x157>(e)};
    f32x2 o = {0.f, 0.f}, o2 = o;
#pragma unroll
    for (int w = 0; w < 8; w += 2) { o += ew[w] * a[w]; o2 += ew[w + 1] * a[w + 1]; }
    o += o2;
    const bool odd = lane & 1;
    st_sc1(rws, aoff, pack4(odd ? M : o.x, odd ? ls : o.y, dpp<XOR1>(o.x), dpp<XOR1>(o.y), km, tg));
    keep_ring();
  } else {
    keep_ring();
  }

  if (is_ffn) {
    static_assert(GL <= 54 && GLX <= 54, "waves 0, 1 read their fragments (first half of gate) in front of the late weights");
    if (wave >= 2) {
      gu_dma(72 - gl - 2, 72, 6);
      const unsigned char* db = (const unsigned char*)(wd + (size_t)fidx * 32 * D);
      for (int d = wave - 2; d < 32; d += 6) dma16(db + d * 1152 + lane * 16, L_D + d * PITCH);
      if (wave >= 4) { const int row = (wave - 4) * 8 + (lane >> 3); dma16(db + row * 1152 + 1024 + (((lane & 7) ^ (2 * ((row >> 1) & 3))) * 16), L_DT + (wave - 4) * 1024); }
      asm volatile("s_waitcnt vmcnt(0)" ::: "memory");
      lds_inc(F_LAND);
      while (flag(F_LAND) < 6) {}
      FENCE();
    }
    const int mt = wave >> 2, kh = (wave >> 1) & 1, tl = wave & 1;
    u32x4 bg[9], bd[6];
    {
      const int g = lane >> 4, q = (lane & 15) >> 2, p = lane & 3;
      const int r0 = 288 * kh + 8 * g + q;
      LAS unsigned char* a0 = L + (mt ? L_U : L_G) + r0 * 64 + ((tl ^ (g & 1)) * 32) + p * 8;
#pragma unroll
      for (int i = 0; i < 9; ++i) {
        const u32x2 u0 = trread(a0 + i * 2048), u1 = trread(a0 + i * 2048 + 256);
        bg[i] = u32x4{u0.x, u0.y, u1.x, u1.y};
      }
    }
    if (wave >= 2) {
#pragma unroll
      for (int i = 0; i < 5; ++i) bd[i] = bfrag576<false>(L, L_D, wave - 2 + 6 * i, 0, lane);
      bd[5] = wave < 4 ? bfrag576<false>(L, L_D, wave + 28, 0, lane) : bfrag576<true>(L, L_DT, wave - 4, 0, lane);
    }
    int o4 = (lane & 1) ? OOB : WS_OP + fidx * 128 + (lane & 14) * 8 + (wave - 2 + 6 * (lane >> 4)) * OTS;
    int o1 = (lane & 33) ? OOB : o4 + 24 * OTS;
    int a_xn = L_XN + kh * 576 + (lane & 1) * 1152 + (lane >> 4) * 16, a_gw = L_GU + wave * 128 + (lane & 15) * 8;
    int a_gr = L_GU + (lane & 15) * 8 + (lane >> 5) * 4 + (lane & 16) * 8;
    int a_aw = L_ACT + wave * 128 + lane * 2, a_ar = L_ACT + wave * 128 + (lane & 1) * 64 + (lane >> 4) * 16;
    PIN(o4); PIN(o1); PIN(a_xn); PIN(a_gw); PIN(a_gr); PIN(a_aw); PIN(a_ar);
    if (wave < 2) {
      int off[3]; u32x4 v[3];
      off[0] = WS_HP + j * HRS + (lane >> 1) * 128 + wave * 32 + (lane & 1) * 16; off[1] = off[0] + 32 * 128; off[2] = lane < 16 ? off[0] + 64 * 128 : OOB;
      int a_x0 = L_XN + wave * 1152 + lane * 8, a_x2 = lane < 16 ? a_x0 + 1024 : L_TRASH + lane * 8;
      PIN(a_x0); PIN(a_x2);
      while (flag(F_LAND) < 1) __builtin_amdgcn_s_sleep(1);
      {
        const int o_[1] = {WS_HP + j * HRS + (fidx >> 1) * 128 + wave * 32 + (lane & 1) * 16}; u32x4 v_[1];
        gather<1, false>(rws, o_, v_, tg);
      }
      gather<3, false>(rws, off, v, tg, lane < 16);
      const unsigned hp[6] = {v[0].x, v[0].z, v[1].x, v[1].z, v[2].x, v[2].z};
      float s1 = 0.f, s2 = 0.f, s1b = 0.f, s2b = 0.f;
#pragma unroll
      for (int i = 0; i < 6; i += 2) {
        s1 = dot2(hp[i], 0x3f803f80u, s1); s2 = dot2(hp[i], hp[i], s2);
        s1b = dot2(hp[i + 1], 0x3f803f80u, s1b); s2b = dot2(hp[i + 1], hp[i + 1], s2b);
      }
      float st = pair32(s1 + s1b, s2 + s2b);
      st = row_sum(pair16(st, st)) * (1.0f / D);
      const float mean = __uint_as_float(__builtin_amdgcn_readlane(__float_as_uint(st), 0)), msq = __uint_as_float(__builtin_amdgcn_readlane(__float_as_uint(st), 32));
      const unsigned mv = cvtpk_a(mean, fmx(fmaf(-mean, mean, msq), 0.f));
      const float nmb = -lo16(mv), den = rbf(__builtin_amdgcn_sqrtf(rbf(hi16(mv) + 1e-5f))), rden = __builtin_amdgcn_rcpf(den);
      unsigned e[6], o[6];
#pragma unroll
      for (int i = 0; i < 6; ++i) { const f32x2 t = f32x2{lo16(hp[i]), hi16(hp[i])} + nmb; e[i] = cvtpk_a(t.x, t.y); }
#pragma unroll
      for (int i = 0; i < 6; ++i) { const f32x2 t = f32x2{lo16(e[i]), hi16(e[i])} * rden; o[i] = cvtpk(t.x, t.y); }
      *(LAS u32x2*)(L + a_x0) = u32x2{o[0], o[1]}; *(LAS u32x2*)(L + a_x0 + 512) = u32x2{o[2], o[3]};
      *(LAS u32x2*)(L + a_x2) = u32x2{o[4], o[5]};
    }
    BAR();
    {
      u32x4 af[9];
#pragma unroll
      for (int i = 0; i < 9; ++i) af[i] = *(LAS u32x4*)(L + a_xn + i * 64);
      SCHED();
      f32x4 c = {0.f, 0.f, 0.f, 0.f}, c2 = c;
#pragma unroll
      for (int i = 0; i < 8; i += 2) { c = mfma(af[i], bg[i], c); c2 = mfma(af[i + 1], bg[i + 1], c2); }
      c = mfma(af[8], bg[8], c);
      *(LAS f32x2*)(L + a_gw) = f32x2{c[0] + c2[0], c[1] + c2[1]};
    }
    BAR();
    if (wave >= 2) {
      LAS float* gu = (LAS float*)(L + a_gr);
      const float g0 = gu[0], g1 = gu[64], u0 = gu[128], u1 = gu[192];
      SCHED();
      const unsigned gb = cvtpk_a(g0 + g1, u0 + u1);
      const float g = lo16(gb), u = hi16(gb);
      constexpr float K0 = -2.0f * LOG2E * 0.7978845608028654f;
      const float dn = 1.0f + __builtin_amdgcn_exp2f(g * fmaf(g * g, K0 * 0.044715f, K0));
      const float ge = rbf(g * __builtin_amdgcn_rcpf(dn));
      *(LAS u16*)(L + a_aw) = (u16)cvtpk(ge * u, 0.f);
      LGKM0();
      const u32x4 a = *(LAS u32x4*)(L + a_ar);
      const f32x4 z = {0.f, 0.f, 0.f, 0.f};
      f32x4 c[6];
#pragma unroll
      for (int i = 0; i < 6; ++i) c[i] = mfma(a, bd[i], z);
      publish_tiles<6>(rws, c, o4, o1, km, lane, tg);
    }
    return;
  }
  if (is_wo) {
    u32x4 bw[5][2];
#pragma unroll
    for (int ks = 0; ks < 2; ++ks) {
#pragma unroll
      for (int i = 0; i < 4; ++i) bw[i][ks] = bfrag576<false>(L, L_WO, wave + 8 * i, ks, lane);
      bw[4][ks] = bfrag576<true>(L, L_WOT, wave & 3, ks, lane);
    }
    const int yb = WS_YP + (b * 32 + hw) * 128 + (lane & 14) * 8;
    int o4 = (lane & 1) ? OOB : yb + (wave + 8 * (lane >> 4)) * YTS;
    int o1 = ((lane & 49) || wave >= 4) ? OOB : yb + (32 + wave) * YTS;
    int a_or = L_O16 + (lane & 1) * 128 + (lane >> 4) * 16;
    PIN(o4); PIN(o1); PIN(a_or);
    if (wave == 0) {
      int off[4]; u32x4 v[4];
      const int base = WS_AP + ((hw * 8 + (lane >> 5) * 4 + ((lane >> 4) & 1)) * 2 + b) * 384;
      off[0] = base + (lane & 15) * 16; off[1] = off[0] + 4 * 384; off[2] = base + 256; off[3] = base + 256 + 4 * 384;
      int a_ow = L_O16 + (lane >> 5) * 128 + (lane & 15) * 8 + ((lane >> 4) & 1) * 4;
      PIN(a_ow);
      gather<4, true>(rws, off, v, tg);
      const float m0 = __uint_as_float(v[2].x), m1 = __uint_as_float(v[3].x);
      const float M = x16_max(fmx(m0, m1));
      const float e0 = __builtin_amdgcn_exp2f((m0 - M) * LOG2E), e1 = __builtin_amdgcn_exp2f((m1 - M) * LOG2E);
      const float lp = fmaf(e0, __uint_as_float(v[2].y), e1 * __uint_as_float(v[3].y));
      const float rl = __builtin_amdgcn_rcpf(pair16(lp, lp));
      const f32x4 o = e0 * __builtin_bit_cast(f32x4, v[0]) + e1 * __builtin_bit_cast(f32x4, v[1]);
      *(LAS unsigned*)(L + a_ow) = cvtpk(pair16(o[0], o[2]) * rl, pair16(o[1], o[3]) * rl);
    }
    BAR();
    const u32x4 a0 = *(LAS u32x4*)(L + a_or), a1 = *(LAS u32x4*)(L + a_or + 64);
    const f32x4 z = {0.f, 0.f, 0.f, 0.f};
    f32x4 c[5];
#pragma unroll
    for (int i = 0; i < 4; ++i) c[i] = mfma(a0, bw[i][0], z);
#pragma unroll
    for (int i = 0; i < 4; ++i) c[i] = mfma(a1, bw[i][1], c[i]);
    SCHED();
    c[4] = mfma(a1, bw[4][1], mfma(a0, bw[4][0], z));
    publish_tiles<5>(rws, c, o4, o1, km, lane, tg);
  }
  if (rho >= 0 && wave == 0) {
    const int uo = (lane >> 2) * 128 + (rho & 1) * 64 + (lane & 3) * 16;
    float xf = lo16(xr), hf;
    int oh = (lane & 1) ? OOB : WS_HP + (((lane >> 2) & 3) + (lane >> 5) * 4) * HRS + rho * 128 + (lane & 16) * 2 + (lane & 2) * 8;
    int oo = (lane & 12) ? OOB : rcol * 2, oe = (rho == 0 && lane == 0) ? WS_EPOCH : OOB;
    PIN(xf); PIN(oh); PIN(oo); PIN(oe);
    {
      int off[4]; u32x4 v[4];
#pragma unroll
      for (int i = 0; i < 4; ++i) off[i] = WS_YP + (rho >> 1) * YTS + i * 2048 + uo;
      gather<4, true>(rws, off, v, tg);
      const unsigned hb = cvtpk(xf + rbf(reduce_units<4>(v)), 0.f);
      hf = lo16(hb);
      auto sw = __builtin_amdgcn_permlane32_swap(hb, hb, false, false);
      const unsigned pk = sw[0] | (sw[1] << 16);
      st_sc1(rws, oh, u32x4{pk, tg.a, dppu<XOR1>(pk), tg.b});
    }
    {
      int off[9]; u32x4 v[9];
#pragma unroll
      for (int i = 0; i < 9; ++i) off[i] = WS_OP + (rho >> 1) * OTS + i * 2048 + uo;
      const rsrc_t ro = mkrsrc(out, 2 * D * 2);
      {
        const int o_[1] = {off[4] - (lane >> 2) * 128}; u32x4 v_[1];
        gather<1, true>(rws, o_, v_, tg);
      }
      gather<9, true>(rws, off, v, tg);
      const unsigned r = cvtpk(hf + rbf(reduce_units<9>(v)), 0.f);
      __builtin_amdgcn_raw_buffer_store_b16((u16)r, ro, oo, 0, 0);
      __builtin_amdgcn_raw_buffer_store_b64(u32x2{tg.a, tg.b}, rws, oe, 0, 16);
    }
  }
}

extern "C" void kernel_launch(void* const* d_in, const int* in_sizes, int n_in, void* d_out, int out_size, void* d_ws, size_t ws_size, hipStream_t stream) {
  (void)hipFuncSetAttribute((const void*)fused_block, hipFuncAttributeMaxDynamicSharedMemorySize, L_END);
  unsigned char* ws = (unsigned char*)d_ws + ((ws_size - ((size_t)4 << 20)) & ~(size_t)0x1fffff);
  fused_block<<<NWG, NTHR, L_END, stream>>>((const u16*)d_in[0], (const u16*)d_in[1], (const u16*)d_in[2], (const u16*)d_in[3], (const u16*)d_in[4],
                                           (const u16*)d_in[6], (const u16*)d_in[7], (const u16*)d_in[8], (u16*)d_out, ws);
}
```

```cpp
#include <hip/hip_runtime.h>
#include <stdint.h>

typedef unsigned short u16;
typedef unsigned u32x4 __attribute__((ext_vector_type(4)));
typedef unsigned u32x2 __attribute__((ext_vector_type(2)));
typedef float f32x4 __attribute__((ext_vector_type(4)));
typedef float f32x2 __attribute__((ext_vector_type(2)));
typedef short s16x4 __attribute__((ext_vector_type(4)));
typedef __bf16 bf16x2 __attribute__((ext_vector_type(2)));
typedef __bf16 bf16x8 __attribute__((ext_vector_type(8)));
typedef __amdgpu_buffer_rsrc_t rsrc_t;
#define LAS __attribute__((address_space(3)))

constexpr int D = 576, H = 32, SK = 8192, DH = 128, DF = 4608;
constexpr int NWG = 256, NTHR = 512;
constexpr int TPC = 4, CBYTES = 4096, NCH = 128;
constexpr unsigned kSpin = 1u << 17;
constexpr int OOB = 0x40000000;
constexpr int QDELAY = 25;
constexpr int GL = 38, GLX = 22;
constexpr int TEND = 3650, SLOPE = 134 * 256 / 5;
constexpr int TEND2 = 3960, SLOPE2 = 146 * 256 / 5;
constexpr int STAG = 60;

constexpr int HRS = 73 * 128, YTS = 65 * 128, OTS = 145 * 128;
constexpr int WS_EPOCH = 0;
constexpr int WS_HP = 4096;
constexpr int WS_AP = 81920;
constexpr int WS_QP = WS_AP + 256 * 2 * 384;
constexpr int WS_YP = WS_QP + 32 * 8 * 2 * 1024;
constexpr int WS_OP = WS_YP + 36 * YTS;
constexpr int WS_END = WS_OP + 36 * OTS;

constexpr int PITCH = 1056;
constexpr int L_WO = 0, L_WOT = 64 * PITCH;
constexpr int L_G = 0, L_U = 36864, L_D = 73728, L_DT = L_D + 32 * PITCH;
constexpr int L_RED = L_DT + 32 * 128;
constexpr int L_Q16 = L_RED + 4096;
constexpr int L_O16 = L_Q16 + 256;
constexpr int L_XN = L_O16 + 256;
constexpr int L_GU = L_XN + 2304;
constexpr int L_ACT = L_GU + 1024;
constexpr int L_ML = L_ACT + 1024;
constexpr int L_TRASH = L_ML + 64;
constexpr int L_CTR = L_TRASH + 512;
constexpr int L_END = L_CTR + 64;
static_assert(L_WOT + 64 * 128 <= L_RED && L_END > 80 * 1024, "one workgroup per CU");
enum { F_QCNT, F_QRDY, F_POLL, F_T0, F_LAND, F_VOTE };

__device__ __forceinline__ float lo16(unsigned u) { return __uint_as_float(u << 16); }
__device__ __forceinline__ float hi16(unsigned u) { return __uint_as_float(u & 0xffff0000u); }
__device__ __forceinline__ unsigned cvtpk(float lo, float hi) {
  bf16x2 r; r.x = (__bf16)lo; r.y = (__bf16)hi; return __builtin_bit_cast(unsigned, r);
}
__device__ __forceinline__ unsigned cvtpk_a(float lo, float hi) {
  unsigned r; asm("v_cvt_pk_bf16_f32 %0, %1, %2" : "=v"(r) : "v"(lo), "v"(hi)); return r;
}
__device__ __forceinline__ float rbf(float f) { return lo16(cvtpk(f, f)); }
__device__ __forceinline__ float fmx(float a, float b) {
  float r; asm("v_max_f32 %0, %1, %2" : "=v"(r) : "v"(a), "v"(b)); return r;
}
__device__ __forceinline__ float fmx3(float a, float b, float c) {
  float r; asm("v_max3_f32 %0, %1, %2, %3" : "=v"(r) : "v"(a), "v"(b), "v"(c)); return r;
}
__device__ __forceinline__ float max8(float x) {
  float r;
  asm("s_nop 1\n\tv_max_f32_dpp %0, %1, %1 row_half_mirror row_mask:0xf bank_mask:0xf\n\ts_nop 1\n\t"
      "v_max_f32_dpp %0, %0, %0 quad_perm:[2,3,0,1] row_mask:0xf bank_mask:0xf\n\ts_nop 1\n\t"
      "v_max_f32_dpp %0, %0, %0 quad_perm:[1,0,3,2] row_mask:0xf bank_mask:0xf" : "=&v"(r) : "v"(x));
  return r;
}
__device__ __forceinline__ float dot2(unsigned k, unsigned q, float acc) {
  return __builtin_amdgcn_fdot2_f32_bf16(__builtin_bit_cast(bf16x2, k), __builtin_bit_cast(bf16x2, q), acc, false);
}
template <int CTRL> __device__ __forceinline__ float dpp(float x) {
  return __builtin_bit_cast(float, __builtin_amdgcn_mov_dpp(__builtin_bit_cast(int, x), CTRL, 0xf, 0xf, true));
}
template <int CTRL> __device__ __forceinline__ unsigned dppu(unsigned x) {
  return (unsigned)__builtin_amdgcn_mov_dpp((int)x, CTRL, 0xf, 0xf, true);
}
constexpr int XOR1 = 0xB1, XOR2 = 0x4E, XOR7 = 0x141, XOR8 = 0x128, ROR4 = 0x124;
__device__ __forceinline__ float pair32(float a, float b) {
  auto s = __builtin_amdgcn_permlane32_swap(__float_as_uint(a), __float_as_uint(b), false, false);
  return __uint_as_float(s[0]) + __uint_as_float(s[1]);
}
__device__ __forceinline__ float pair16(float a, float b) {
  auto s = __builtin_amdgcn_permlane16_swap(__float_as_uint(a), __float_as_uint(b), false, false);
  return __uint_as_float(s[0]) + __uint_as_float(s[1]);
}
__device__ __forceinline__ float x16_max(float x) {
  auto s = __builtin_amdgcn_permlane16_swap(__float_as_uint(x), __float_as_uint(x), false, false);
  return fmx(__uint_as_float(s[0]), __uint_as_float(s[1]));
}
__device__ __forceinline__ float x32_max(float x) {
  auto s = __builtin_amdgcn_permlane32_swap(__float_as_uint(x), __float_as_uint(x), false, false);
  return fmx(__uint_as_float(s[0]), __uint_as_float(s[1]));
}
__device__ __forceinline__ float row_sum(float d) { d += dpp<XOR8>(d); d += dpp<XOR7>(d); d += dpp<XOR2>(d); d += dpp<XOR1>(d); return d; }
__device__ __forceinline__ float fdiv(float a, float b, float rb) {
  const float q = a * rb; return fmaf(fmaf(-q, b, a), rb, q);
}
#define BAR() asm volatile("s_waitcnt lgkmcnt(0)\n\ts_barrier" ::: "memory")
#define LGKM0() asm volatile("s_waitcnt lgkmcnt(0)" ::: "memory")
#define FENCE() asm volatile("" ::: "memory")
#define PIN(x) asm volatile("" : "+v"(x))
#define SCHED() __builtin_amdgcn_sched_barrier(0)
__device__ __forceinline__ void dma16(const void* gsrc, unsigned lds_dst) {
  unsigned keep;
  asm volatile("s_mov_b32 %0, m0\n\ts_mov_b32 m0, %2\n\ts_nop 0\n\tglobal_load_lds_dwordx4 %1, off nt\n\ts_mov_b32 m0, %0" : "=&s"(keep) : "v"(gsrc), "s"(lds_dst) : "memory");
}
__device__ __forceinline__ rsrc_t mkrsrc(const void* p, int bytes) { return __builtin_amdgcn_make_buffer_rsrc((void*)p, 0, bytes, 0x00020000); }
__device__ __forceinline__ u32x4 ld_sc1(rsrc_t r, int off) { return __builtin_amdgcn_raw_buffer_load_b128(r, off, 0, 16); }
__device__ __forceinline__ void st_sc1(rsrc_t r, int off, u32x4 v) { __builtin_amdgcn_raw_buffer_store_b128(v, r, off, 0, 16); }
__device__ __forceinline__ u32x4 ld_nt(rsrc_t r, int off) { return __builtin_amdgcn_raw_buffer_load_b128(r, off, 0, 2); }
__device__ __forceinline__ u32x2 trread(LAS unsigned char* p) {
  return __builtin_bit_cast(u32x2, __builtin_amdgcn_ds_read_tr16_b64_v4i16((LAS s16x4*)p));
}
__device__ __forceinline__ f32x4 mfma(u32x4 a, u32x4 b, f32x4 c) {
  return __builtin_amdgcn_mfma_f32_16x16x32_bf16(__builtin_bit_cast(bf16x8, a), __builtin_bit_cast(bf16x8, b), c, 0, 0, 0);
}

struct Tags { unsigned a, b, p0, p1, p2, p3; };
__device__ __forceinline__ u32x4 pack4(float v0, float v1, float v2, float v3, unsigned km, const Tags& t) {
  return u32x4{(__float_as_uint(v0) & km) | t.p0, (__float_as_uint(v1) & km) | t.p1, (__float_as_uint(v2) & km) | t.p2, (__float_as_uint(v3) & km) | t.p3};
}
__device__ __forceinline__ unsigned orxor(unsigned acc, unsigned x, unsigned t) { return __builtin_amdgcn_bitop3_b32(acc, x, t, 0xF6); }
template <int N, bool PACKED>
__device__ __forceinline__ void gather(rsrc_t rs, const int (&off)[N], u32x4 (&v)[N], const Tags& t, bool last = true) {
  for (unsigned spins = 0; spins < kSpin; ++spins) {
    FENCE();
#pragma unroll
    for (int i = 0; i < N; ++i) v[i] = ld_sc1(rs, off[i]);
    unsigned bad = 0;
#pragma unroll
    for (int i = 0; i < N; ++i) {
      if (i == N - 1 && N == 3) break;
      if (PACKED) { v[i].x ^= t.p0; v[i].y ^= t.p1; v[i].z ^= t.p2; v[i].w ^= t.p3; bad |= (v[i].x | v[i].y) | (v[i].z | v[i].w); }
      else bad = orxor(orxor(bad, v[i].y, t.a), v[i].w, t.b);
    }
    if (N == 3) bad |= last ? (v[N - 1].y ^ t.a) | (v[N - 1].w ^ t.b) : 0u;
    if (__builtin_amdgcn_ballot_w64((PACKED ? bad << 20 : bad) != 0u) == 0ull) break;
  }
}
template <bool TAIL>
__device__ __forceinline__ u32x4 bfrag576(LAS unsigned char* L, int base, int T, int ks, int lane) {
  const int g = lane >> 4, q = (lane & 15) >> 2, p = lane & 3;
  const int r0 = 32 * ks + 8 * g + q;
  u32x2 x, y;
  if (!TAIL) { LAS unsigned char* a = L + base + r0 * PITCH + T * 32 + p * 8; x = trread(a); y = trread(a + 4 * PITCH); }
  else { x = trread(L + base + r0 * 128 + ((T ^ ((r0 >> 1) & 3)) * 32) + p * 8); y = trread(L + base + (r0 + 4) * 128 + ((T ^ (((r0 + 4) >> 1) & 3)) * 32) + p * 8); }
  return u32x4{x.x, x.y, y.x, y.y};
}
template <int NT>
__device__ __forceinline__ void publish_tiles(rsrc_t rws, const f32x4 (&c)[NT], int o4, int o1, unsigned km, int lane, const Tags& t) {
  const bool g1 = lane & 16, g2 = lane & 32;
  const float a0 = g1 ? c[1][0] : c[0][0], b0 = g1 ? c[3][0] : c[2][0], a1 = g1 ? c[1][1] : c[0][1], b1 = g1 ? c[3][1] : c[2][1];
  const float s0 = g2 ? b0 : a0, s1 = g2 ? b1 : a1;
  st_sc1(rws, o4, pack4(s0, s1, dpp<XOR1>(s0), dpp<XOR1>(s1), km, t));
  const float r0 = g1 ? c[NT - 1][0] : c[4][0], r1 = g1 ? c[NT - 1][1] : c[4][1];
  st_sc1(rws, o1, pack4(r0, r1, dpp<XOR1>(r0), dpp<XOR1>(r1), km, t));
}
template <int N>
__device__ __forceinline__ float reduce_units(const u32x4 (&v)[N]) {
  f32x4 s = __builtin_bit_cast(f32x4, v[0]), s2 = __builtin_bit_cast(f32x4, v[1]);
#pragma unroll
  for (int i = 2; i < N; ++i) { if (i & 1) s2 += __builtin_bit_cast(f32x4, v[i]); else s += __builtin_bit_cast(f32x4, v[i]); }
  s += s2;
  float y = pair16(pair32(s[0], s[2]), pair32(s[1], s[3]));
  y += dpp<XOR8>(y);
  return y + dpp<ROR4>(y);
}

__global__ void __launch_bounds__(NTHR, 2)
fused_block(const u16* __restrict__ x, const u16* __restrict__ kc, const u16* __restrict__ vc, const u16* __restrict__ wq, const u16* __restrict__ wo,
            const u16* __restrict__ wg, const u16* __restrict__ wu, const u16* __restrict__ wd, u16* __restrict__ out, unsigned char* __restrict__ ws) {
  extern __shared__ __attribute__((aligned(16))) unsigned char smem[];
  LAS unsigned char* L = (LAS unsigned char*)smem;
  const int tid = threadIdx.x, lane = tid & 63, wave = __builtin_amdgcn_readfirstlane(tid >> 6);
  const int blk = blockIdx.x, h = blk >> 3, j = blk & 7, b = j >> 2, s = j & 3;
  asm volatile("" :: "s"(x), "s"(kc), "s"(vc), "s"(wq), "s"(ws));
  const rsrc_t rws = mkrsrc(ws, WS_END);
  u32x2 ep;
  Tags tg;
  auto tags = [&]() {
    PIN(ep);
    tg.a = __builtin_amdgcn_readfirstlane(ep.x) + 0x9E3779B1u; tg.b = __builtin_amdgcn_readfirstlane(ep.y) + 0x85EBCA77u;
    tg.p0 = tg.a & 0xfffu; tg.p1 = (tg.a >> 12) & 0xfffu; tg.p2 = tg.b & 0xfffu; tg.p3 = (tg.b >> 12) & 0xfffu;
  };
  volatile LAS int* flags = (volatile LAS int*)(L + L_CTR);
  auto flag = [&](int i) { return __builtin_amdgcn_readfirstlane(flags[i]); };
  auto lds_inc = [&](int i) { if (lane == 0) __hip_atomic_fetch_add((LAS int*)(L + L_CTR) + i, 1, __ATOMIC_RELAXED, __HIP_MEMORY_SCOPE_WORKGROUP); };
  u32x4 qv[8];
  const int qo = WS_QP + (h * 16 + b) * 1024;
  if (wave < 4) {
    ep = __builtin_amdgcn_raw_buffer_load_b64(rws, WS_EPOCH, 0, 16);
    BAR();
  } else {
    __builtin_amdgcn_s_setprio(3);
    if (lane < 16) ((LAS int*)(L + L_CTR))[lane] = 0;
    const rsrc_t rx = mkrsrc(x, 2 * D * 2), rq = mkrsrc(wq + h * DH, D * H * DH * 2 - h * DH * 2);
    const u32x4 xa = __builtin_amdgcn_raw_buffer_load_b128(rx, lane * 36, 0, 0);
    const u32x4 xb = __builtin_amdgcn_raw_buffer_load_b128(rx, lane * 36 + 16, 0, 0);
    const unsigned xc = __builtin_amdgcn_raw_buffer_load_b32(rx, lane * 36 + 32, 0, 0);
    const unsigned xq = __builtin_amdgcn_raw_buffer_load_b32(rx, (lane >> 5) * 1152 + ((lane & 15) < 8 ? lane & 15 : 8) * 4, (36 * j + 9 * (wave - 4)) * 4, 0);
    const int r0 = (72 * j + 18 * (wave - 4)) * (H * DH * 2);
    unsigned wv[18];
#pragma unroll
    for (int i = 0; i < 18; ++i) wv[i] = __builtin_amdgcn_raw_buffer_load_b32(rq, lane * 4, r0 + i * (H * DH * 2), 2);
    ep = __builtin_amdgcn_raw_buffer_load_b64(rws, WS_EPOCH, 0, 16);
    SCHED();
    BAR();
    if (wave == 4) flags[F_T0] = (int)(unsigned)__builtin_amdgcn_s_memrealtime();
    SCHED();
    LAS f32x2* red = (LAS f32x2*)(L + L_RED);
    const unsigned xd[9] = {xa.x, xa.y, xa.z, xa.w, xb.x, xb.y, xb.z, xb.w, xc};
    float s1 = 0.f, s2 = 0.f;
#pragma unroll
    for (int i = 0; i < 9; ++i) { s1 = dot2(xd[i], 0x3f803f80u, s1); s2 = dot2(xd[i], xd[i], s2); }
    const float st = row_sum(pair16(s1, s2)) * (1.0f / D);
    auto sw = __builtin_amdgcn_permlane16_swap(__float_as_uint(st), __float_as_uint(st), false, false);
    const float mean = __uint_as_float(sw[0]), msq = __uint_as_float(sw[1]);
    const unsigned mv = cvtpk_a(mean, fmx(fmaf(-mean, mean, msq), 0.f));
    const float den = rbf(__builtin_amdgcn_sqrtf(rbf(hi16(mv) + 1e-5f))), rden = __builtin_amdgcn_rcpf(den);
    const unsigned e = cvtpk_a(lo16(xq) - lo16(mv), hi16(xq) - lo16(mv));
    const unsigned xn = cvtpk_a(fdiv(lo16(e), den, rden), fdiv(hi16(e), den, rden));
    float a00 = 0.f, a01 = 0.f, a10 = 0.f, a11 = 0.f;
#pragma unroll
    for (int i = 0; i < 9; ++i) {
      const unsigned wl = __builtin_amdgcn_perm(wv[2 * i + 1], wv[2 * i], 0x05040100u), wh = __builtin_amdgcn_perm(wv[2 * i + 1], wv[2 * i], 0x07060302u);
      const unsigned x0 = __builtin_amdgcn_readlane(xn, i), x1 = __builtin_amdgcn_readlane(xn, 32 + i);
      a00 = dot2(wl, x0, a00); a01 = dot2(wh, x0, a01); a10 = dot2(wl, x1, a10); a11 = dot2(wh, x1, a11);
    }
    red[((wave - 4) * 2 + 0) * 64 + lane] = f32x2{a00, a01};
    red[((wave - 4) * 2 + 1) * 64 + lane] = f32x2{a10, a11};
    tags();
    LGKM0();
    lds_inc(F_QCNT);
    if (wave < 6) {
      while (flag(F_QCNT) < 4) {}
      FENCE();
      f32x2 t = red[(wave - 4) * 64 + lane];
#pragma unroll
      for (int w = 1; w < 4; ++w) t += red[(w * 2 + wave - 4) * 64 + lane];
      st_sc1(rws, WS_QP + ((h * 8 + j) * 2 + wave - 4) * 1024 + lane * 16, u32x4{__float_as_uint(t.x), tg.a, __float_as_uint(t.y), tg.b});
    }
    if (wave == 4) {
      __builtin_amdgcn_s_sleep(QDELAY);
      FENCE();
#pragma unroll
      for (int p = 0; p < 8; ++p) qv[p] = __builtin_amdgcn_raw_buffer_load_b128(rws, lane * 16, qo + p * 2048, 16);
      FENCE();
      flags[F_POLL] = 1;
    } else {
      __builtin_amdgcn_s_setprio(0);
      if (wave == 7) { while (flag(F_POLL) == 0) __builtin_amdgcn_s_sleep(1); }
    }
  }
  SCHED();
  const size_t kvo = (size_t)(b * 32 + h) * SK * DH;
  const rsrc_t rk = mkrsrc(kc + kvo, SK * DH * 2), rv = mkrsrc(vc + kvo, SK * DH * 2);
  int vo = (s * 8 + wave) * CBYTES + lane * 16 + ((h & 15) << 17);
  u32x4 K[TPC];
#pragma unroll
  for (int t = 0; t < TPC; ++t) { K[t] = ld_nt(rk, vo + t * 1024); SCHED(); }
  if (wave == 4) {
    unsigned bad = 0;
#pragma unroll
    for (int p = 0; p < 8; ++p) bad = orxor(orxor(bad, qv[p].y, tg.a), qv[p].w, tg.b);
    if (__builtin_amdgcn_ballot_w64(bad != 0u) != 0ull) {
      int qoff[8];
#pragma unroll
      for (int p = 0; p < 8; ++p) qoff[p] = qo + p * 2048 + lane * 16;
      gather<8, false>(rws, qoff, qv, tg);
    }
    float q0 = 0.f, q1 = 0.f;
#pragma unroll
    for (int p = 0; p < 8; ++p) { q0 += __uint_as_float(qv[p].x); q1 += __uint_as_float(qv[p].z); }
    ((LAS unsigned*)(L + L_Q16))[lane] = cvtpk(q0, q1);
    LGKM0();
    flags[F_QRDY] = 1;
    __builtin_amdgcn_s_setprio(0);
  }
  SCHED();
  const int rr = (s - h) & 3, hq = (h >> 2) & 3;
  const bool is_wo = (rr == 0 && hq != 3) || (rr == 3 && hq == 2), is_ffn = rr == 1 || rr == 2 || (rr == 3 && hq == 3);
  const int hw = rr == 0 ? h : h + 4;
  const int fidx = rr == 1 ? 2 * (b * 32 + h) : rr == 2 ? 2 * (b * 32 + ((h + 1) & 31)) + 1 : 128 + 2 * (b * 4 + (h & 3)) + (h >> 4);
  const int gl = (((int)((size_t)wg >> 7) + (fidx >> 1)) & 7) == 3 ? GLX : GL;
  auto gu_dma = [&](int p0, int p1, int n) {
    const int sw = ((lane & 3) ^ (2 * ((lane >> 5) & 1))) * 8, r = lane >> 2;
    for (int p = p0 + wave; p < p1; p += n) {
      const int q = p >= 36 ? p - 36 : p;
      dma16((p >= 36 ? wu : wg) + (size_t)(q * 16 + r) * DF + fidx * 32 + sw, L_G + p * 1024);
    }
  };
  if (wave < 4) {
    if (is_wo) {
      const unsigned char* wb = (const unsigned char*)(wo + (size_t)(hw * DH + b * 64) * D);
#pragma unroll 4
      for (int i = 0; i < 16; ++i) dma16(wb + (wave * 16 + i) * 1152 + lane * 16, L_WO + (wave * 16 + i) * PITCH);
#pragma unroll
      for (int i = 0; i < 2; ++i) {
        const int row = wave * 16 + i * 8 + (lane >> 3);
        dma16(wb + row * 1152 + 1024 + (((lane & 7) ^ (2 * ((row >> 1) & 3))) * 16), L_WOT + (wave * 2 + i) * 1024);
      }
    } else if (is_ffn) {
      gu_dma(0, 72 - gl, 4);
    }
  }
  SCHED();
  tags();
  const int rho = (rr == 3 && hq < 2) ? b * 16 + (h >> 4) * 8 + (h & 3) * 2 + hq : (rr == 0 && hq == 3) ? 32 + b * 8 + (h & 3) * 2 + (h >> 4)
                : (rr == 0 && hq < 2 && (b == 0 || h < 8)) ? 48 + b * 16 + (h >> 4) * 8 + (h & 3) * 2 + hq : -1;
  const int rcol = (lane & 16) * (D / 16) + 8 * rho + 2 * (lane & 3) + (lane >> 5);
  const unsigned xr = __builtin_amdgcn_raw_buffer_load_b16(mkrsrc(x, 2 * D * 2), (rho >= 0 && wave == 0) ? rcol * 2 : OOB, 0, 0);
  while (flag(F_QRDY) == 0) __builtin_amdgcn_s_sleep(1);
  FENCE();
  const u32x4 qp = *(LAS u32x4*)(L + L_Q16 + (lane & 15) * 16);

  constexpr float SCALE = 0.088388347648318440f, LOG2E = 1.4426950408889634f;
  float m = -3.0e38f, l = 0.f, acc[8];
#pragma unroll
  for (int k = 0; k < 8; ++k) acc[k] = 0.f;
  int aoff = (lane & 30) * 8;
  aoff = (lane & 1) ? OOB : aoff;
  aoff = (lane & 31) == 1 ? 256 : aoff;
  aoff += WS_AP + (blk * 2 + (lane >> 5)) * 384;
  int a_mw = L_RED + wave * 512 + (lane & 15) * 32 + (lane >> 4) * 8, a_mr = L_RED + lane * 8, a_ml = L_ML + (lane & 7) * 8;
  unsigned km = 0xfffff000u;
  PIN(aoff); PIN(a_mw); PIN(a_mr); PIN(a_ml); PIN(km);
  const unsigned t0 = (unsigned)flag(F_T0);
  int send = TEND + ((((h * 13) & 31) * STAG) >> 5) - STAG / 2, slope = SLOPE, prev = 0;
  for (int left = NCH - 8 - wave; left > -8; left -= 8) {
    unsigned now = (unsigned)__builtin_amdgcn_s_memrealtime();
    bool tight = true;
    float sc[TPC];
#pragma unroll
    for (int t = 0; t < TPC; ++t) {
      float d = dot2(K[t].x, qp.x, 0.f); d = dot2(K[t].y, qp.y, d); d = dot2(K[t].z, qp.z, d); d = dot2(K[t].w, qp.w, d);
      PIN(d);
      K[t] = ld_nt(rv, vo + t * 1024);
      SCHED();
      sc[t] = row_sum(d);
    }
    float pm = m;
#pragma unroll
    for (int t = 0; t < TPC; t += 2) {
      const unsigned r1 = cvtpk_a(sc[t], sc[t + 1]);
      const unsigned r2 = cvtpk_a(lo16(r1) * SCALE, hi16(r1) * SCALE);
      sc[t] = lo16(r2); sc[t + 1] = hi16(r2);
      pm = fmx3(pm, sc[t], sc[t + 1]);
    }
    const float alpha = __builtin_amdgcn_exp2f((m - pm) * LOG2E);
    m = pm; l *= alpha;
    const float mo = -pm * LOG2E;
#pragma unroll
    for (int k = 0; k < 8; ++k) acc[k] *= alpha;
#pragma unroll
    for (int t = 0; t < TPC; ++t) { sc[t] = __builtin_amdgcn_exp2f(fmaf(sc[t], LOG2E, mo)); l += sc[t]; }
    vo = ((vo + 32 * CBYTES) & 0x1fffff) | (left > 0 ? 0 : OOB);
#pragma unroll
    for (int t = 0; t < TPC; ++t) {
      const float p = sc[t];
      acc[0] = fmaf(p, lo16(K[t].x), acc[0]); acc[1] = fmaf(p, hi16(K[t].x), acc[1]);
      acc[2] = fmaf(p, lo16(K[t].y), acc[2]); acc[3] = fmaf(p, hi16(K[t].y), acc[3]);
      acc[4] = fmaf(p, lo16(K[t].z), acc[4]); acc[5] = fmaf(p, hi16(K[t].z), acc[5]);
      acc[6] = fmaf(p, lo16(K[t].w), acc[6]); acc[7] = fmaf(p, hi16(K[t].w), acc[7]);
      asm volatile("" : "+v"(acc[0]), "+v"(acc[1]), "+v"(acc[2]), "+v"(acc[3]), "+v"(acc[4]), "+v"(acc[5]), "+v"(acc[6]), "+v"(acc[7]));
      if (t == 0) {
        asm volatile("" : "+s"(now));
        now -= t0;
        const int due = left > 0 ? send - (((left > 3 ? left : 3) * slope) >> 8) : 0;
        tight = (int)now - prev >= (8 * SLOPE >> 8) - 10;
        prev = (int)now;
        if ((int)now < due) {
          now = (unsigned)__builtin_amdgcn_s_memrealtime() - t0;
          tight = tight && due - (int)now < 25;
          while ((int)now < due) { __builtin_amdgcn_s_sleep(4); now = (unsigned)__builtin_amdgcn_s_memrealtime() - t0; }
        }
      }
      K[t] = ld_nt(rk, vo + t * 1024);
      SCHED();
    }
    if (left <= NCH - 32 && left > NCH - 64 && slope == SLOPE) {
      if (tight && left > NCH - 48) lds_inc(F_VOTE);
      if (flag(F_VOTE) >= 10) { send += TEND2 - TEND; slope = SLOPE2; }
    }
  }
  auto keep_ring = [&]() {
    SCHED();
#pragma unroll
    for (int t = 0; t < TPC; ++t) asm volatile("" :: "v"(K[t]));
  };
  {
    const float M = x32_max(x16_max(m));
    const float w = __builtin_amdgcn_exp2f((m - M) * LOG2E);
    float a[9];
#pragma unroll
    for (int k = 0; k < 8; ++k) a[k] = acc[k] * w;
    a[8] = l * w;
    SCHED();
    const float t0_ = pair32(a[0], a[4]), t1 = pair32(a[1], a[5]), t2 = pair32(a[2], a[6]), t3 = pair32(a[3], a[7]), t4 = pair32(a[8], a[8]);
    SCHED();
    const float u0 = pair16(t0_, t2), u1 = pair16(t1, t3), ls = pair16(t4, t4);
    *(LAS f32x2*)(L + a_mw) = f32x2{u0, u1};
    *(LAS f32x2*)(L + L_ML + wave * 8) = f32x2{M, ls};
  }
  BAR();
  if (wave == 0) {
    const f32x2 ml = *(LAS f32x2*)(L + a_ml);
    f32x2 a[8];
#pragma unroll
    for (int w = 0; w < 8; ++w) a[w] = *(LAS f32x2*)(L + a_mr + w * 512);
    SCHED();
    const float M = max8(ml.x);
    const float e = __builtin_amdgcn_exp2f((ml.x - M) * LOG2E);
    float ls = e * ml.y;
    ls += dpp<XOR7>(ls); ls += dpp<XOR2>(ls); ls += dpp<XOR1>(ls);
    const float ew[8] = {dpp<0x150>(e), dpp<0x151>(e), dpp<0x152>(e), dpp<0x153>(e), dpp<0x154>(e), dpp<0x155>(e), dpp<0x156>(e), dpp<0x157>(e)};
    f32x2 o = {0.f, 0.f}, o2 = o;
#pragma unroll
    for (int w = 0; w < 8; w += 2) { o += ew[w] * a[w]; o2 += ew[w + 1] * a[w + 1]; }
    o += o2;
    const bool odd = lane & 1;
    st_sc1(rws, aoff, pack4(odd ? M : o.x, odd ? ls : o.y, dpp<XOR1>(o.x), dpp<XOR1>(o.y), km, tg));
    keep_ring();
  } else {
    keep_ring();
  }

  if (is_ffn) {
    static_assert(GL <= 54 && GLX <= 54, "waves 0, 1 read their fragments (first half of gate) in front of the late weights");
    if (wave >= 2) {
      gu_dma(72 - gl - 2, 72, 6);
      const unsigned char* db = (const unsigned char*)(wd + (size_t)fidx * 32 * D);
      for (int d = wave - 2; d < 32; d += 6) dma16(db + d * 1152 + lane * 16, L_D + d * PITCH);
      if (wave >= 4) { const int row = (wave - 4) * 8 + (lane >> 3); dma16(db + row * 1152 + 1024 + (((lane & 7) ^ (2 * ((row >> 1) & 3))) * 16), L_DT + (wave - 4) * 1024); }
      asm volatile("s_waitcnt vmcnt(0)" ::: "memory");
      lds_inc(F_LAND);
      while (flag(F_LAND) < 6) {}
      FENCE();
    }
    const int mt = wave >> 2, kh = (wave >> 1) & 1, tl = wave & 1;
    u32x4 bg[9], bd[6];
    {
      const int g = lane >> 4, q = (lane & 15) >> 2, p = lane & 3;
      const int r0 = 288 * kh + 8 * g + q;
      LAS unsigned char* a0 = L + (mt ? L_U : L_G) + r0 * 64 + ((tl ^ (g & 1)) * 32) + p * 8;
#pragma unroll
      for (int i = 0; i < 9; ++i) {
        const u32x2 u0 = trread(a0 + i * 2048), u1 = trread(a0 + i * 2048 + 256);
        bg[i] = u32x4{u0.x, u0.y, u1.x, u1.y};
      }
    }
    if (wave >= 2) {
#pragma unroll
      for (int i = 0; i < 5; ++i) bd[i] = bfrag576<false>(L, L_D, wave - 2 + 6 * i, 0, lane);
      bd[5] = wave < 4 ? bfrag576<false>(L, L_D, wave + 28, 0, lane) : bfrag576<true>(L, L_DT, wave - 4, 0, lane);
    }
    int o4 = (lane & 1) ? OOB : WS_OP + fidx * 128 + (lane & 14) * 8 + (wave - 2 + 6 * (lane >> 4)) * OTS;
    int o1 = (lane & 33) ? OOB : o4 + 24 * OTS;
    int a_xn = L_XN + kh * 576 + (lane & 1) * 1152 + (lane >> 4) * 16, a_gw = L_GU + wave * 128 + (lane & 15) * 8;
    int a_gr = L_GU + (lane & 15) * 8 + (lane >> 5) * 4 + (lane & 16) * 8;
    int a_aw = L_ACT + wave * 128 + lane * 2, a_ar = L_ACT + wave * 128 + (lane & 1) * 64 + (lane >> 4) * 16;
    PIN(o4); PIN(o1); PIN(a_xn); PIN(a_gw); PIN(a_gr); PIN(a_aw); PIN(a_ar);
    if (wave < 2) {
      int off[3]; u32x4 v[3];
      off[0] = WS_HP + j * HRS + (lane >> 1) * 128 + wave * 32 + (lane & 1) * 16; off[1] = off[0] + 32 * 128; off[2] = lane < 16 ? off[0] + 64 * 128 : OOB;
      int a_x0 = L_XN + wave * 1152 + lane * 8, a_x2 = lane < 16 ? a_x0 + 1024 : L_TRASH + lane * 8;
      PIN(a_x0); PIN(a_x2);
      while (flag(F_LAND) < 1) __builtin_amdgcn_s_sleep(1);
      {
        const int o_[1] = {WS_HP + j * HRS + (fidx >> 1) * 128 + wave * 32 + (lane & 1) * 16}; u32x4 v_[1];
        gather<1, false>(rws, o_, v_, tg);
      }
      gather<3, false>(rws, off, v, tg, lane < 16);
      const unsigned hp[6] = {v[0].x, v[0].z, v[1].x, v[1].z, v[2].x, v[2].z};
      float s1 = 0.f, s2 = 0.f, s1b = 0.f, s2b = 0.f;
#pragma unroll
      for (int i = 0; i < 6; i += 2) {
        s1 = dot2(hp[i], 0x3f803f80u, s1); s2 = dot2(hp[i], hp[i], s2);
        s1b = dot2(hp[i + 1], 0x3f803f80u, s1b); s2b = dot2(hp[i + 1], hp[i + 1], s2b);
      }
      float st = pair32(s1 + s1b, s2 + s2b);
      st = row_sum(pair16(st, st)) * (1.0f / D);
      const float mean = __uint_as_float(__builtin_amdgcn_readlane(__float_as_uint(st), 0)), msq = __uint_as_float(__builtin_amdgcn_readlane(__float_as_uint(st), 32));
      const unsigned mv = cvtpk_a(mean, fmx(fmaf(-mean, mean, msq), 0.f));
      const float nmb = -lo16(mv), den = rbf(__builtin_amdgcn_sqrtf(rbf(hi16(mv) + 1e-5f))), rden = __builtin_amdgcn_rcpf(den);
      unsigned e[6], o[6];
#pragma unroll
      for (int i = 0; i < 6; ++i) { const f32x2 t = f32x2{lo16(hp[i]), hi16(hp[i])} + nmb; e[i] = cvtpk_a(t.x, t.y); }
#pragma unroll
      for (int i = 0; i < 6; ++i) { const f32x2 t = f32x2{lo16(e[i]), hi16(e[i])} * rden; o[i] = cvtpk(t.x, t.y); }
      *(LAS u32x2*)(L + a_x0) = u32x2{o[0], o[1]}; *(LAS u32x2*)(L + a_x0 + 512) = u32x2{o[2], o[3]};
      *(LAS u32x2*)(L + a_x2) = u32x2{o[4], o[5]};
    }
    BAR();
    {
      u32x4 af[9];
#pragma unroll
      for (int i = 0; i < 9; ++i) af[i] = *(LAS u32x4*)(L + a_xn + i * 64);
      SCHED();
      f32x4 c = {0.f, 0.f, 0.f, 0.f}, c2 = c;
#pragma unroll
      for (int i = 0; i < 8; i += 2) { c = mfma(af[i], bg[i], c); c2 = mfma(af[i + 1], bg[i + 1], c2); }
      c = mfma(af[8], bg[8], c);
      *(LAS f32x2*)(L + a_gw) = f32x2{c[0] + c2[0], c[1] + c2[1]};
    }
    BAR();
    if (wave >= 2) {
      LAS float* gu = (LAS float*)(L + a_gr);
      const float g0 = gu[0], g1 = gu[64], u0 = gu[128], u1 = gu[192];
      SCHED();
      const unsigned gb = cvtpk_a(g0 + g1, u0 + u1);
      const float g = lo16(gb), u = hi16(gb);
      constexpr float K0 = -2.0f * LOG2E * 0.7978845608028654f;
      const float dn = 1.0f + __builtin_amdgcn_exp2f(g * fmaf(g * g, K0 * 0.044715f, K0));
      const float ge = rbf(g * __builtin_amdgcn_rcpf(dn));
      *(LAS u16*)(L + a_aw) = (u16)cvtpk(ge * u, 0.f);
      LGKM0();
      const u32x4 a = *(LAS u32x4*)(L + a_ar);
      const f32x4 z = {0.f, 0.f, 0.f, 0.f};
      f32x4 c[6];
#pragma unroll
      for (int i = 0; i < 6; ++i) c[i] = mfma(a, bd[i], z);
      publish_tiles<6>(rws, c, o4, o1, km, lane, tg);
    }
    return;
  }
  if (is_wo) {
    u32x4 bw[5][2];
#pragma unroll
    for (int ks = 0; ks < 2; ++ks) {
#pragma unroll
      for (int i = 0; i < 4; ++i) bw[i][ks] = bfrag576<false>(L, L_WO, wave + 8 * i, ks, lane);
      bw[4][ks] = bfrag576<true>(L, L_WOT, wave & 3, ks, lane);
    }
    const int yb = WS_YP + (b * 32 + hw) * 128 + (lane & 14) * 8;
    int o4 = (lane & 1) ? OOB : yb + (wave + 8 * (lane >> 4)) * YTS;
    int o1 = ((lane & 49) || wave >= 4) ? OOB : yb + (32 + wave) * YTS;
    int a_or = L_O16 + (lane & 1) * 128 + (lane >> 4) * 16;
    PIN(o4); PIN(o1); PIN(a_or);
    if (wave == 0) {
      int off[4]; u32x4 v[4];
      const int base = WS_AP + ((hw * 8 + (lane >> 5) * 4 + ((lane >> 4) & 1)) * 2 + b) * 384;
      off[0] = base + (lane & 15) * 16; off[1] = off[0] + 4 * 384; off[2] = base + 256; off[3] = base + 256 + 4 * 384;
      int a_ow = L_O16 + (lane >> 5) * 128 + (lane & 15) * 8 + ((lane >> 4) & 1) * 4;
      PIN(a_ow);
      gather<4, true>(rws, off, v, tg);
      const float m0 = __uint_as_float(v[2].x), m1 = __uint_as_float(v[3].x);
      const float M = x16_max(fmx(m0, m1));
      const float e0 = __builtin_amdgcn_exp2f((m0 - M) * LOG2E), e1 = __builtin_amdgcn_exp2f((m1 - M) * LOG2E);
      const float lp = fmaf(e0, __uint_as_float(v[2].y), e1 * __uint_as_float(v[3].y));
      const float rl = __builtin_amdgcn_rcpf(pair16(lp, lp));
      const f32x4 o = e0 * __builtin_bit_cast(f32x4, v[0]) + e1 * __builtin_bit_cast(f32x4, v[1]);
      *(LAS unsigned*)(L + a_ow) = cvtpk(pair16(o[0], o[2]) * rl, pair16(o[1], o[3]) * rl);
    }
    BAR();
    const u32x4 a0 = *(LAS u32x4*)(L + a_or), a1 = *(LAS u32x4*)(L + a_or + 64);
    const f32x4 z = {0.f, 0.f, 0.f, 0.f};
    f32x4 c[5];
#pragma unroll
    for (int i = 0; i < 4; ++i) c[i] = mfma(a0, bw[i][0], z);
#pragma unroll
    for (int i = 0; i < 4; ++i) c[i] = mfma(a1, bw[i][1], c[i]);
    SCHED();
    c[4] = mfma(a1, bw[4][1], mfma(a0, bw[4][0], z));
    publish_tiles<5>(rws, c, o4, o1, km, lane, tg);
  }
  if (rho >= 0 && wave == 0) {
    const int uo = (lane >> 2) * 128 + (rho & 1) * 64 + (lane & 3) * 16;
    float xf = lo16(xr), hf;
    int oh = (lane & 1) ? OOB : WS_HP + (((lane >> 2) & 3) + (lane >> 5) * 4) * HRS + rho * 128 + (lane & 16) * 2 + (lane & 2) * 8;
    int oo = (lane & 12) ? OOB : rcol * 2, oe = (rho == 0 && lane == 0) ? WS_EPOCH : OOB;
    PIN(xf); PIN(oh); PIN(oo); PIN(oe);
    {
      int off[4]; u32x4 v[4];
#pragma unroll
      for (int i = 0; i < 4; ++i) off[i] = WS_YP + (rho >> 1) * YTS + i * 2048 + uo;
      gather<4, true>(rws, off, v, tg);
      const unsigned hb = cvtpk(xf + rbf(reduce_units<4>(v)), 0.f);
      hf = lo16(hb);
      auto sw = __builtin_amdgcn_permlane32_swap(hb, hb, false, false);
      const unsigned pk = sw[0] | (sw[1] << 16);
      st_sc1(rws, oh, u32x4{pk, tg.a, dppu<XOR1>(pk), tg.b});
    }
    {
      int off[9]; u32x4 v[9];
#pragma unroll
      for (int i = 0; i < 9; ++i) off[i] = WS_OP + (rho >> 1) * OTS + i * 2048 + uo;
      const rsrc_t ro = mkrsrc(out, 2 * D * 2);
      {
        const int o_[1] = {off[4] - (lane >> 2) * 128}; u32x4 v_[1];
        gather<1, true>(rws, o_, v_, tg);
      }
      gather<9, true>(rws, off, v, tg);
      const unsigned r = cvtpk(hf + rbf(reduce_units<9>(v)), 0.f);
      __builtin_amdgcn_raw_buffer_store_b16((u16)r, ro, oo, 0, 0);
      __builtin_amdgcn_raw_buffer_store_b64(u32x2{tg.a, tg.b}, rws, oe, 0, 16);
    }
  }
}

extern "C" void kernel_launch(void* const* d_in, const int* in_sizes, int n_in, void* d_out, int out_size, void* d_ws, size_t ws_size, hipStream_t stream) {
  (void)hipFuncSetAttribute((const void*)fused_block, hipFuncAttributeMaxDynamicSharedMemorySize, L_END);
  unsigned char* ws = (unsigned char*)d_ws + ((ws_size - ((size_t)4 << 20)) & ~(size_t)0x1fffff);
  fused_block<<<NWG, NTHR, L_END, stream>>>((const u16*)d_in[0], (const u16*)d_in[1], (const u16*)d_in[2], (const u16*)d_in[3], (const u16*)d_in[4],
                                           (const u16*)d_in[6], (const u16*)d_in[7], (const u16*)d_in[8], (u16*)d_out, ws);
}
```

```cpp
#include <hip/hip_runtime.h>
#include <stdint.h>

typedef unsigned short u16;
typedef unsigned u32x4 __attribute__((ext_vector_type(4)));
typedef unsigned u32x2 __attribute__((ext_vector_type(2)));
typedef float f32x4 __attribute__((ext_vector_type(4)));
typedef float f32x2 __attribute__((ext_vector_type(2)));
typedef short s16x4 __attribute__((ext_vector_type(4)));
typedef __bf16 bf16x2 __attribute__((ext_vector_type(2)));
typedef __bf16 bf16x8 __attribute__((ext_vector_type(8)));
typedef __amdgpu_buffer_rsrc_t rsrc_t;
#define LAS __attribute__((address_space(3)))

constexpr int D = 576, H = 32, SK = 8192, DH = 128, DF = 4608;
constexpr int NWG = 256, NTHR = 512;
constexpr int TPC = 4, CBYTES = 4096, NCH = 128;
constexpr unsigned kSpin = 1u << 17;
constexpr int OOB = 0x40000000;
constexpr int QDELAY = 25;
constexpr int GL = 38, GLX = 22;
constexpr int TEND = 3670, SLOPE = 134 * 256 / 10;
constexpr int STAG = 85;

constexpr int HRS = 73 * 128, YTS = 65 * 128, OTS = 145 * 128;
constexpr int WS_EPOCH = 0;
constexpr int WS_HP = 4096;
constexpr int WS_AP = 81920;
constexpr int WS_QP = WS_AP + 256 * 2 * 384;
constexpr int WS_YP = WS_QP + 32 * 8 * 2 * 1024;
constexpr int WS_OP = WS_YP + 36 * YTS;
constexpr int WS_END = WS_OP + 36 * OTS;

constexpr int PITCH = 1056;
constexpr int L_WO = 0, L_WOT = 64 * PITCH;
constexpr int L_G = 0, L_U = 36864, L_D = 73728, L_DT = L_D + 32 * PITCH;
constexpr int L_RED = L_DT + 32 * 128;
constexpr int L_Q16 = L_RED + 4096;
constexpr int L_O16 = L_Q16 + 256;
constexpr int L_XN = L_O16 + 256;
constexpr int L_GU = L_XN + 2304;
constexpr int L_ACT = L_GU + 1024;
constexpr int L_ML = L_ACT + 1024;
constexpr int L_TRASH = L_ML + 64;
constexpr int L_SC = L_TRASH + 512;
constexpr int L_CTR = L_SC + 8192;
constexpr int L_END = L_CTR + 64;
static_assert(L_WOT + 64 * 128 <= L_RED && L_END > 80 * 1024, "one workgroup per CU");
enum { F_QCNT, F_QRDY, F_POLL, F_T0, F_LAND, F_MAX };

__device__ __forceinline__ float lo16(unsigned u) { return __uint_as_float(u << 16); }
__device__ __forceinline__ float hi16(unsigned u) { return __uint_as_float(u & 0xffff0000u); }
__device__ __forceinline__ unsigned cvtpk(float lo, float hi) {
  bf16x2 r; r.x = (__bf16)lo; r.y = (__bf16)hi; return __builtin_bit_cast(unsigned, r);
}
__device__ __forceinline__ unsigned cvtpk_a(float lo, float hi) {
  unsigned r; asm("v_cvt_pk_bf16_f32 %0, %1, %2" : "=v"(r) : "v"(lo), "v"(hi)); return r;
}
__device__ __forceinline__ float rbf(float f) { return lo16(cvtpk(f, f)); }
__device__ __forceinline__ float fmx(float a, float b) {
  float r; asm("v_max_f32 %0, %1, %2" : "=v"(r) : "v"(a), "v"(b)); return r;
}
__device__ __forceinline__ float fmx3(float a, float b, float c) {
  float r; asm("v_max3_f32 %0, %1, %2, %3" : "=v"(r) : "v"(a), "v"(b), "v"(c)); return r;
}
__device__ __forceinline__ float max8(float x) {
  float r;
  asm("s_nop 1\n\tv_max_f32_dpp %0, %1, %1 row_half_mirror row_mask:0xf bank_mask:0xf\n\ts_nop 1\n\t"
      "v_max_f32_dpp %0, %0, %0 quad_perm:[2,3,0,1] row_mask:0xf bank_mask:0xf\n\ts_nop 1\n\t"
      "v_max_f32_dpp %0, %0, %0 quad_perm:[1,0,3,2] row_mask:0xf bank_mask:0xf" : "=&v"(r) : "v"(x));
  return r;
}
__device__ __forceinline__ float dot2(unsigned k, unsigned q, float acc) {
  return __builtin_amdgcn_fdot2_f32_bf16(__builtin_bit_cast(bf16x2, k), __builtin_bit_cast(bf16x2, q), acc, false);
}
template <int CTRL> __device__ __forceinline__ float dpp(float x) {
  return __builtin_bit_cast(float, __builtin_amdgcn_mov_dpp(__builtin_bit_cast(int, x), CTRL, 0xf, 0xf, true));
}
template <int CTRL> __device__ __forceinline__ unsigned dppu(unsigned x) {
  return (unsigned)__builtin_amdgcn_mov_dpp((int)x, CTRL, 0xf, 0xf, true);
}
constexpr int XOR1 = 0xB1, XOR2 = 0x4E, XOR7 = 0x141, XOR8 = 0x128, ROR4 = 0x124;
__device__ __forceinline__ float pair32(float a, float b) {
  auto s = __builtin_amdgcn_permlane32_swap(__float_as_uint(a), __float_as_uint(b), false, false);
  return __uint_as_float(s[0]) + __uint_as_float(s[1]);
}
__device__ __forceinline__ float pair16(float a, float b) {
  auto s = __builtin_amdgcn_permlane16_swap(__float_as_uint(a), __float_as_uint(b), false, false);
  return __uint_as_float(s[0]) + __uint_as_float(s[1]);
}
__device__ __forceinline__ float x16_max(float x) {
  auto s = __builtin_amdgcn_permlane16_swap(__float_as_uint(x), __float_as_uint(x), false, false);
  return fmx(__uint_as_float(s[0]), __uint_as_float(s[1]));
}
__device__ __forceinline__ float x32_max(float x) {
  auto s = __builtin_amdgcn_permlane32_swap(__float_as_uint(x), __float_as_uint(x), false, false);
  return fmx(__uint_as_float(s[0]), __uint_as_float(s[1]));
}
__device__ __forceinline__ float row_sum(float d) { d += dpp<XOR8>(d); d += dpp<XOR7>(d); d += dpp<XOR2>(d); d += dpp<XOR1>(d); return d; }
__device__ __forceinline__ float fdiv(float a, float b, float rb) {
  const float q = a * rb; return fmaf(fmaf(-q, b, a), rb, q);
}
#define BAR() asm volatile("s_waitcnt lgkmcnt(0)\n\ts_barrier" ::: "memory")
#define LGKM0() asm volatile("s_waitcnt lgkmcnt(0)" ::: "memory")
#define FENCE() asm volatile("" ::: "memory")
#define PIN(x) asm volatile("" : "+v"(x))
#define SCHED() __builtin_amdgcn_sched_barrier(0)
__device__ __forceinline__ void dma16(const void* gsrc, unsigned lds_dst) {
  unsigned keep;
  asm volatile("s_mov_b32 %0, m0\n\ts_mov_b32 m0, %2\n\ts_nop 0\n\tglobal_load_lds_dwordx4 %1, off nt\n\ts_mov_b32 m0, %0" : "=&s"(keep) : "v"(gsrc), "s"(lds_dst) : "memory");
}
__device__ __forceinline__ rsrc_t mkrsrc(const void* p, int bytes) { return __builtin_amdgcn_make_buffer_rsrc((void*)p, 0, bytes, 0x00020000); }
__device__ __forceinline__ u32x4 ld_sc1(rsrc_t r, int off) { return __builtin_amdgcn_raw_buffer_load_b128(r, off, 0, 16); }
__device__ __forceinline__ void st_sc1(rsrc_t r, int off, u32x4 v) { __builtin_amdgcn_raw_buffer_store_b128(v, r, off, 0, 16); }
__device__ __forceinline__ u32x4 ld_nt(rsrc_t r, int off) { return __builtin_amdgcn_raw_buffer_load_b128(r, off, 0, 2); }
__device__ __forceinline__ u32x2 trread(LAS unsigned char* p) {
  return __builtin_bit_cast(u32x2, __builtin_amdgcn_ds_read_tr16_b64_v4i16((LAS s16x4*)p));
}
__device__ __forceinline__ f32x4 mfma(u32x4 a, u32x4 b, f32x4 c) {
  return __builtin_amdgcn_mfma_f32_16x16x32_bf16(__builtin_bit_cast(bf16x8, a), __builtin_bit_cast(bf16x8, b), c, 0, 0, 0);
}

struct Tags { unsigned a, b, p0, p1, p2, p3; };
__device__ __forceinline__ u32x4 pack4(float v0, float v1, float v2, float v3, unsigned km, const Tags& t) {
  return u32x4{(__float_as_uint(v0) & km) | t.p0, (__float_as_uint(v1) & km) | t.p1, (__float_as_uint(v2) & km) | t.p2, (__float_as_uint(v3) & km) | t.p3};
}
__device__ __forceinline__ unsigned orxor(unsigned acc, unsigned x, unsigned t) { return __builtin_amdgcn_bitop3_b32(acc, x, t, 0xF6); }
template <int N, bool PACKED>
__device__ __forceinline__ void gather(rsrc_t rs, const int (&off)[N], u32x4 (&v)[N], const Tags& t, bool last = true) {
  for (unsigned spins = 0; spins < kSpin; ++spins) {
    FENCE();
#pragma unroll
    for (int i = 0; i < N; ++i) v[i] = ld_sc1(rs, off[i]);
    unsigned bad = 0;
#pragma unroll
    for (int i = 0; i < N; ++i) {
      if (i == N - 1 && N == 3) break;
      if (PACKED) { v[i].x ^= t.p0; v[i].y ^= t.p1; v[i].z ^= t.p2; v[i].w ^= t.p3; bad |= (v[i].x | v[i].y) | (v[i].z | v[i].w); }
      else bad = orxor(orxor(bad, v[i].y, t.a), v[i].w, t.b);
    }
    if (N == 3) bad |= last ? (v[N - 1].y ^ t.a) | (v[N - 1].w ^ t.b) : 0u;
    if (__builtin_amdgcn_ballot_w64((PACKED ? bad << 20 : bad) != 0u) == 0ull) break;
  }
}
template <bool TAIL>
__device__ __forceinline__ u32x4 bfrag576(LAS unsigned char* L, int base, int T, int ks, int lane) {
  const int g = lane >> 4, q = (lane & 15) >> 2, p = lane & 3;
  const int r0 = 32 * ks + 8 * g + q;
  u32x2 x, y;
  if (!TAIL) { LAS unsigned char* a = L + base + r0 * PITCH + T * 32 + p * 8; x = trread(a); y = trread(a + 4 * PITCH); }
  else { x = trread(L + base + r0 * 128 + ((T ^ ((r0 >> 1) & 3)) * 32) + p * 8); y = trread(L + base + (r0 + 4) * 128 + ((T ^ (((r0 + 4) >> 1) & 3)) * 32) + p * 8); }
  return u32x4{x.x, x.y, y.x, y.y};
}
template <int NT>
__device__ __forceinline__ void publish_tiles(rsrc_t rws, const f32x4 (&c)[NT], int o4, int o1, unsigned km, int lane, const Tags& t) {
  const bool g1 = lane & 16, g2 = lane & 32;
  const float a0 = g1 ? c[1][0] : c[0][0], b0 = g1 ? c[3][0] : c[2][0], a1 = g1 ? c[1][1] : c[0][1], b1 = g1 ? c[3][1] : c[2][1];
  const float s0 = g2 ? b0 : a0, s1 = g2 ? b1 : a1;
  st_sc1(rws, o4, pack4(s0, s1, dpp<XOR1>(s0), dpp<XOR1>(s1), km, t));
  const float r0 = g1 ? c[NT - 1][0] : c[4][0], r1 = g1 ? c[NT - 1][1] : c[4][1];
  st_sc1(rws, o1, pack4(r0, r1, dpp<XOR1>(r0), dpp<XOR1>(r1), km, t));
}
template <int N>
__device__ __forceinline__ float reduce_units(const u32x4 (&v)[N]) {
  f32x4 s = __builtin_bit_cast(f32x4, v[0]), s2 = __builtin_bit_cast(f32x4, v[1]);
#pragma unroll
  for (int i = 2; i < N; ++i) { if (i & 1) s2 += __builtin_bit_cast(f32x4, v[i]); else s += __builtin_bit_cast(f32x4, v[i]); }
  s += s2;
  float y = pair16(pair32(s[0], s[2]), pair32(s[1], s[3]));
  y += dpp<XOR8>(y);
  return y + dpp<ROR4>(y);
}

__global__ void __launch_bounds__(NTHR, 2)
fused_block(const u16* __restrict__ x, const u16* __restrict__ kc, const u16* __restrict__ vc, const u16* __restrict__ wq, const u16* __restrict__ wo,
            const u16* __restrict__ wg, const u16* __restrict__ wu, const u16* __restrict__ wd, u16* __restrict__ out, unsigned char* __restrict__ ws) {
  extern __shared__ __attribute__((aligned(16))) unsigned char smem[];
  LAS unsigned char* L = (LAS unsigned char*)smem;
  const int tid = threadIdx.x, lane = tid & 63, wave = __builtin_amdgcn_readfirstlane(tid >> 6);
  const int blk = blockIdx.x, h = blk >> 3, j = blk & 7, b = j >> 2, s = j & 3;
  asm volatile("" :: "s"(x), "s"(kc), "s"(vc), "s"(wq), "s"(ws));
  const rsrc_t rws = mkrsrc(ws, WS_END);
  u32x2 ep;
  Tags tg;
  auto tags = [&]() {
    PIN(ep);
    tg.a = __builtin_amdgcn_readfirstlane(ep.x) + 0x9E3779B1u; tg.b = __builtin_amdgcn_readfirstlane(ep.y) + 0x85EBCA77u;
    tg.p0 = tg.a & 0xfffu; tg.p1 = (tg.a >> 12) & 0xfffu; tg.p2 = tg.b & 0xfffu; tg.p3 = (tg.b >> 12) & 0xfffu;
  };
  volatile LAS int* flags = (volatile LAS int*)(L + L_CTR);
  auto flag = [&](int i) { return __builtin_amdgcn_readfirstlane(flags[i]); };
  auto lds_inc = [&](int i) { if (lane == 0) __hip_atomic_fetch_add((LAS int*)(L + L_CTR) + i, 1, __ATOMIC_RELAXED, __HIP_MEMORY_SCOPE_WORKGROUP); };
  u32x4 qv[8];
  const int qo = WS_QP + (h * 16 + b) * 1024;
  if (wave < 4) {
    ep = __builtin_amdgcn_raw_buffer_load_b64(rws, WS_EPOCH, 0, 16);
    BAR();
  } else {
    __builtin_amdgcn_s_setprio(3);
    if (lane < 16) ((LAS int*)(L + L_CTR))[lane] = 0;
    const rsrc_t rx = mkrsrc(x, 2 * D * 2), rq = mkrsrc(wq + h * DH, D * H * DH * 2 - h * DH * 2);
    const u32x4 xa = __builtin_amdgcn_raw_buffer_load_b128(rx, lane * 36, 0, 0);
    const u32x4 xb = __builtin_amdgcn_raw_buffer_load_b128(rx, lane * 36 + 16, 0, 0);
    const unsigned xc = __builtin_amdgcn_raw_buffer_load_b32(rx, lane * 36 + 32, 0, 0);
    const unsigned xq = __builtin_amdgcn_raw_buffer_load_b32(rx, (lane >> 5) * 1152 + ((lane & 15) < 8 ? lane & 15 : 8) * 4, (36 * j + 9 * (wave - 4)) * 4, 0);
    const int r0 = (72 * j + 18 * (wave - 4)) * (H * DH * 2);
    unsigned wv[18];
#pragma unroll
    for (int i = 0; i < 18; ++i) wv[i] = __builtin_amdgcn_raw_buffer_load_b32(rq, lane * 4, r0 + i * (H * DH * 2), 2);
    ep = __builtin_amdgcn_raw_buffer_load_b64(rws, WS_EPOCH, 0, 16);
    SCHED();
    BAR();
    if (wave == 4) flags[F_T0] = (int)(unsigned)__builtin_amdgcn_s_memrealtime();
    SCHED();
    LAS f32x2* red = (LAS f32x2*)(L + L_RED);
    const unsigned xd[9] = {xa.x, xa.y, xa.z, xa.w, xb.x, xb.y, xb.z, xb.w, xc};
    float s1 = 0.f, s2 = 0.f;
#pragma unroll
    for (int i = 0; i < 9; ++i) { s1 = dot2(xd[i], 0x3f803f80u, s1); s2 = dot2(xd[i], xd[i], s2); }
    const float st = row_sum(pair16(s1, s2)) * (1.0f / D);
    auto sw = __builtin_amdgcn_permlane16_swap(__float_as_uint(st), __float_as_uint(st), false, false);
    const float mean = __uint_as_float(sw[0]), msq = __uint_as_float(sw[1]);
    const unsigned mv = cvtpk_a(mean, fmx(fmaf(-mean, mean, msq), 0.f));
    const float den = rbf(__builtin_amdgcn_sqrtf(rbf(hi16(mv) + 1e-5f))), rden = __builtin_amdgcn_rcpf(den);
    const unsigned e = cvtpk_a(lo16(xq) - lo16(mv), hi16(xq) - lo16(mv));
    const unsigned xn = cvtpk_a(fdiv(lo16(e), den, rden), fdiv(hi16(e), den, rden));
    float a00 = 0.f, a01 = 0.f, a10 = 0.f, a11 = 0.f;
#pragma unroll
    for (int i = 0; i < 9; ++i) {
      const unsigned wl = __builtin_amdgcn_perm(wv[2 * i + 1], wv[2 * i], 0x05040100u), wh = __builtin_amdgcn_perm(wv[2 * i + 1], wv[2 * i], 0x07060302u);
      const unsigned x0 = __builtin_amdgcn_readlane(xn, i), x1 = __builtin_amdgcn_readlane(xn, 32 + i);
      a00 = dot2(wl, x0, a00); a01 = dot2(wh, x0, a01); a10 = dot2(wl, x1, a10); a11 = dot2(wh, x1, a11);
    }
    red[((wave - 4) * 2 + 0) * 64 + lane] = f32x2{a00, a01};
    red[((wave - 4) * 2 + 1) * 64 + lane] = f32x2{a10, a11};
    tags();
    LGKM0();
    lds_inc(F_QCNT);
    if (wave < 6) {
      while (flag(F_QCNT) < 4) {}
      FENCE();
      f32x2 t = red[(wave - 4) * 64 + lane];
#pragma unroll
      for (int w = 1; w < 4; ++w) t += red[(w * 2 + wave - 4) * 64 + lane];
      st_sc1(rws, WS_QP + ((h * 8 + j) * 2 + wave - 4) * 1024 + lane * 16, u32x4{__float_as_uint(t.x), tg.a, __float_as_uint(t.y), tg.b});
    }
    if (wave == 4) {
      __builtin_amdgcn_s_sleep(QDELAY);
      FENCE();
#pragma unroll
      for (int p = 0; p < 8; ++p) qv[p] = __builtin_amdgcn_raw_buffer_load_b128(rws, lane * 16, qo + p * 2048, 16);
      FENCE();
      flags[F_POLL] = 1;
    } else {
      __builtin_amdgcn_s_setprio(0);
      if (wave == 7) { while (flag(F_POLL) == 0) __builtin_amdgcn_s_sleep(1); }
    }
  }
  SCHED();
  const size_t kvo = (size_t)(b * 32 + h) * SK * DH;
  const rsrc_t rk = mkrsrc(kc + kvo, SK * DH * 2), rv = mkrsrc(vc + kvo, SK * DH * 2);
  int vo = (s * 8 + wave) * CBYTES + lane * 16 + ((h & 15) << 17);
  u32x4 K[TPC];
#pragma unroll
  for (int t = 0; t < TPC; ++t) { K[t] = ld_nt(rk, vo + t * 1024); SCHED(); }
  if (wave == 4) {
    unsigned bad = 0;
#pragma unroll
    for (int p = 0; p < 8; ++p) bad = orxor(orxor(bad, qv[p].y, tg.a), qv[p].w, tg.b);
    if (__builtin_amdgcn_ballot_w64(bad != 0u) != 0ull) {
      int qoff[8];
#pragma unroll
      for (int p = 0; p < 8; ++p) qoff[p] = qo + p * 2048 + lane * 16;
      gather<8, false>(rws, qoff, qv, tg);
    }
    float q0 = 0.f, q1 = 0.f;
#pragma unroll
    for (int p = 0; p < 8; ++p) { q0 += __uint_as_float(qv[p].x); q1 += __uint_as_float(qv[p].z); }
    ((LAS unsigned*)(L + L_Q16))[lane] = cvtpk(q0, q1);
    LGKM0();
    flags[F_QRDY] = 1;
    __builtin_amdgcn_s_setprio(0);
  }
  SCHED();
  const int rr = (s - h) & 3, hq = (h >> 2) & 3;
  const bool is_wo = (rr == 0 && hq != 3) || (rr == 3 && hq == 2), is_ffn = rr == 1 || rr == 2 || (rr == 3 && hq == 3);
  const int hw = rr == 0 ? h : h + 4;
  const int fidx = rr == 1 ? 2 * (b * 32 + h) : rr == 2 ? 2 * (b * 32 + ((h + 1) & 31)) + 1 : 128 + 2 * (b * 4 + (h & 3)) + (h >> 4);
  const int gl = (((int)((size_t)wg >> 7) + (fidx >> 1)) & 7) == 3 ? GLX : GL;
  auto gu_dma = [&](int p0, int p1, int n) {
    const int sw = ((lane & 3) ^ (2 * ((lane >> 5) & 1))) * 8, r = lane >> 2;
    for (int p = p0 + wave; p < p1; p += n) {
      const int q = p >= 36 ? p - 36 : p;
      dma16((p >= 36 ? wu : wg) + (size_t)(q * 16 + r) * DF + fidx * 32 + sw, L_G + p * 1024);
    }
  };
  if (wave < 4) {
    if (is_wo) {
      const unsigned char* wb = (const unsigned char*)(wo + (size_t)(hw * DH + b * 64) * D);
#pragma unroll 4
      for (int i = 0; i < 16; ++i) dma16(wb + (wave * 16 + i) * 1152 + lane * 16, L_WO + (wave * 16 + i) * PITCH);
#pragma unroll
      for (int i = 0; i < 2; ++i) {
        const int row = wave * 16 + i * 8 + (lane >> 3);
        dma16(wb + row * 1152 + 1024 + (((lane & 7) ^ (2 * ((row >> 1) & 3))) * 16), L_WOT + (wave * 2 + i) * 1024);
      }
    } else if (is_ffn) {
      gu_dma(0, 72 - gl, 4);
    }
  }
  SCHED();
  tags();
  const int rho = (rr == 3 && hq < 2) ? b * 16 + (h >> 4) * 8 + (h & 3) * 2 + hq : (rr == 0 && hq == 3) ? 32 + b * 8 + (h & 3) * 2 + (h >> 4)
                : (rr == 0 && hq < 2 && (b == 0 || h < 8)) ? 48 + b * 16 + (h >> 4) * 8 + (h & 3) * 2 + hq : -1;
  const int rcol = (lane & 16) * (D / 16) + 8 * rho + 2 * (lane & 3) + (lane >> 5);
  const unsigned xr = __builtin_amdgcn_raw_buffer_load_b16(mkrsrc(x, 2 * D * 2), (rho >= 0 && wave == 0) ? rcol * 2 : OOB, 0, 0);
  while (flag(F_QRDY) == 0) __builtin_amdgcn_s_sleep(1);
  FENCE();
  const u32x4 qp = *(LAS u32x4*)(L + L_Q16 + (lane & 15) * 16);

  constexpr float SCALE = 0.088388347648318440f, LOG2E = 1.4426950408889634f;
  float m = -3.0e38f, l = 0.f, acc[8];
#pragma unroll
  for (int k = 0; k < 8; ++k) acc[k] = 0.f;
  int aoff = (lane & 30) * 8;
  aoff = (lane & 1) ? OOB : aoff;
  aoff = (lane & 31) == 1 ? 256 : aoff;
  aoff += WS_AP + (blk * 2 + (lane >> 5)) * 384;
  int a_mw = L_RED + wave * 512 + (lane & 15) * 32 + (lane >> 4) * 8, a_mr = L_RED + lane * 8, a_ml = L_ML + (lane & 7) * 8;
  unsigned km = 0xfffff000u;
  PIN(aoff); PIN(a_mw); PIN(a_mr); PIN(a_ml); PIN(km);
  const unsigned t0 = (unsigned)flag(F_T0);
  const int send = TEND + ((((h * 13) & 31) * STAG) >> 5) - STAG / 2;
  auto pace = [&](unsigned now, int left) {
    asm volatile("" : "+s"(now));
    now -= t0;
    const int due = send - (((left > 3 ? left : 3) * SLOPE) >> 8);
    if ((int)now < due) {
      now = (unsigned)__builtin_amdgcn_s_memrealtime() - t0;
      while ((int)now < due) { __builtin_amdgcn_s_sleep(2); now = (unsigned)__builtin_amdgcn_s_memrealtime() - t0; }
    }
  };
  int a_sc = L_SC + wave * 1024 + (lane >> 4) * 16;
  for (int k = 0; k < 16; ++k) {
    const unsigned now = (unsigned)__builtin_amdgcn_s_memrealtime();
    SCHED();
    vo = (vo + 32 * CBYTES) & 0x1fffff;
    const rsrc_t rn = k < 15 ? rk : rv;
    float sc[TPC];
#pragma unroll
    for (int t = 0; t < TPC; ++t) {
      float d = dot2(K[t].x, qp.x, 0.f); d = dot2(K[t].y, qp.y, d); d = dot2(K[t].z, qp.z, d); d = dot2(K[t].w, qp.w, d);
      PIN(d);
      if (t == 0) pace(now, 2 * NCH - 8 - wave - 8 * k);
      K[t] = ld_nt(rn, vo + t * 1024);
      SCHED();
      sc[t] = row_sum(d);
    }
#pragma unroll
    for (int t = 0; t < TPC; t += 2) {
      const unsigned r1 = cvtpk_a(sc[t], sc[t + 1]);
      const unsigned r2 = cvtpk_a(lo16(r1) * SCALE, hi16(r1) * SCALE);
      sc[t] = lo16(r2); sc[t + 1] = hi16(r2);
      m = fmx3(m, sc[t], sc[t + 1]);
    }
    if ((lane & 15) == 0) *(LAS f32x4*)(L + a_sc + k * 64) = f32x4{sc[0], sc[1], sc[2], sc[3]};
  }
  m = x32_max(x16_max(m));
  float mo = -m * LOG2E;
  *(LAS float*)(L + L_ML + wave * 8) = m;
  LGKM0();
  lds_inc(F_MAX);
  for (int k = 0; k < 16; ++k) {
    if (k == 2) {
      while (flag(F_MAX) < 8) {}
      FENCE();
      const float M = max8(*(LAS float*)(L + a_ml)), f = __builtin_amdgcn_exp2f((m - M) * LOG2E);
      m = M; mo = -M * LOG2E; l *= f;
#pragma unroll
      for (int i = 0; i < 8; ++i) acc[i] *= f;
    }
    const unsigned now = (unsigned)__builtin_amdgcn_s_memrealtime();
    const f32x4 s4 = *(LAS f32x4*)(L + a_sc + k * 64);
    vo = ((vo + 32 * CBYTES) & 0x1fffff) | (k < 15 ? 0 : OOB);
    float sc[TPC];
#pragma unroll
    for (int t = 0; t < TPC; ++t) { sc[t] = __builtin_amdgcn_exp2f(fmaf(s4[t], LOG2E, mo)); l += sc[t]; }
#pragma unroll
    for (int t = 0; t < TPC; ++t) {
      const float p = sc[t];
      acc[0] = fmaf(p, lo16(K[t].x), acc[0]); acc[1] = fmaf(p, hi16(K[t].x), acc[1]);
      acc[2] = fmaf(p, lo16(K[t].y), acc[2]); acc[3] = fmaf(p, hi16(K[t].y), acc[3]);
      acc[4] = fmaf(p, lo16(K[t].z), acc[4]); acc[5] = fmaf(p, hi16(K[t].z), acc[5]);
      acc[6] = fmaf(p, lo16(K[t].w), acc[6]); acc[7] = fmaf(p, hi16(K[t].w), acc[7]);
      asm volatile("" : "+v"(acc[0]), "+v"(acc[1]), "+v"(acc[2]), "+v"(acc[3]), "+v"(acc[4]), "+v"(acc[5]), "+v"(acc[6]), "+v"(acc[7]));
      if (t == 0 && k < 15) pace(now, NCH - 8 - wave - 8 * k);
      K[t] = ld_nt(rv, vo + t * 1024);
      SCHED();
    }
  }
  auto keep_ring = [&]() {
    SCHED();
#pragma unroll
    for (int t = 0; t < TPC; ++t) asm volatile("" :: "v"(K[t]));
  };
  {
    const float t0_ = pair32(acc[0], acc[4]), t1 = pair32(acc[1], acc[5]), t2 = pair32(acc[2], acc[6]), t3 = pair32(acc[3], acc[7]), t4 = pair32(l, l);
    SCHED();
    const float u0 = pair16(t0_, t2), u1 = pair16(t1, t3), ls = pair16(t4, t4);
    *(LAS f32x2*)(L + a_mw) = f32x2{u0, u1};
    *(LAS float*)(L + L_ML + wave * 8 + 4) = ls;
  }
  BAR();
  if (wave == 0) {
    float ls = *(LAS float*)(L + a_ml + 4);
    f32x2 a[8];
#pragma unroll
    for (int w = 0; w < 8; ++w) a[w] = *(LAS f32x2*)(L + a_mr + w * 512);
    SCHED();
    const f32x2 o = ((a[0] + a[1]) + (a[2] + a[3])) + ((a[4] + a[5]) + (a[6] + a[7]));
    ls += dpp<XOR7>(ls); ls += dpp<XOR2>(ls); ls += dpp<XOR1>(ls);
    const bool odd = lane & 1;
    st_sc1(rws, aoff, pack4(odd ? m : o.x, odd ? ls : o.y, dpp<XOR1>(o.x), dpp<XOR1>(o.y), km, tg));
    keep_ring();
  } else {
    keep_ring();
  }

  if (is_ffn) {
    static_assert(GL <= 54 && GLX <= 54, "waves 0, 1 read their fragments (first half of gate) in front of the late weights");
    if (wave >= 2) {
      gu_dma(72 - gl - 2, 72, 6);
      const unsigned char* db = (const unsigned char*)(wd + (size_t)fidx * 32 * D);
      for (int d = wave - 2; d < 32; d += 6) dma16(db + d * 1152 + lane * 16, L_D + d * PITCH);
      if (wave >= 4) { const int row = (wave - 4) * 8 + (lane >> 3); dma16(db + row * 1152 + 1024 + (((lane & 7) ^ (2 * ((row >> 1) & 3))) * 16), L_DT + (wave - 4) * 1024); }
      asm volatile("s_waitcnt vmcnt(0)" ::: "memory");
      lds_inc(F_LAND);
      while (flag(F_LAND) < 6) {}
      FENCE();
    }
    const int mt = wave >> 2, kh = (wave >> 1) & 1, tl = wave & 1;
    u32x4 bg[9], bd[6];
    {
      const int g = lane >> 4, q = (lane & 15) >> 2, p = lane & 3;
      const int r0 = 288 * kh + 8 * g + q;
      LAS unsigned char* a0 = L + (mt ? L_U : L_G) + r0 * 64 + ((tl ^ (g & 1)) * 32) + p * 8;
#pragma unroll
      for (int i = 0; i < 9; ++i) {
        const u32x2 u0 = trread(a0 + i * 2048), u1 = trread(a0 + i * 2048 + 256);
        bg[i] = u32x4{u0.x, u0.y, u1.x, u1.y};
      }
    }
    if (wave >= 2) {
#pragma unroll
      for (int i = 0; i < 5; ++i) bd[i] = bfrag576<false>(L, L_D, wave - 2 + 6 * i, 0, lane);
      bd[5] = wave < 4 ? bfrag576<false>(L, L_D, wave + 28, 0, lane) : bfrag576<true>(L, L_DT, wave - 4, 0, lane);
    }
    int o4 = (lane & 1) ? OOB : WS_OP + fidx * 128 + (lane & 14) * 8 + (wave - 2 + 6 * (lane >> 4)) * OTS;
    int o1 = (lane & 33) ? OOB : o4 + 24 * OTS;
    int a_xn = L_XN + kh * 576 + (lane & 1) * 1152 + (lane >> 4) * 16, a_gw = L_GU + wave * 128 + (lane & 15) * 8;
    int a_gr = L_GU + (lane & 15) * 8 + (lane >> 5) * 4 + (lane & 16) * 8;
    int a_aw = L_ACT + wave * 128 + lane * 2, a_ar = L_ACT + wave * 128 + (lane & 1) * 64 + (lane >> 4) * 16;
    PIN(o4); PIN(o1); PIN(a_xn); PIN(a_gw); PIN(a_gr); PIN(a_aw); PIN(a_ar);
    if (wave < 2) {
      int off[3]; u32x4 v[3];
      off[0] = WS_HP + j * HRS + (lane >> 1) * 128 + wave * 32 + (lane & 1) * 16; off[1] = off[0] + 32 * 128; off[2] = lane < 16 ? off[0] + 64 * 128 : OOB;
      int a_x0 = L_XN + wave * 1152 + lane * 8, a_x2 = lane < 16 ? a_x0 + 1024 : L_TRASH + lane * 8;
      PIN(a_x0); PIN(a_x2);
      while (flag(F_LAND) < 1) __builtin_amdgcn_s_sleep(1);
      {
        const int o_[1] = {WS_HP + j * HRS + (fidx >> 1) * 128 + wave * 32 + (lane & 1) * 16}; u32x4 v_[1];
        gather<1, false>(rws, o_, v_, tg);
      }
      gather<3, false>(rws, off, v, tg, lane < 16);
      const unsigned hp[6] = {v[0].x, v[0].z, v[1].x, v[1].z, v[2].x, v[2].z};
      float s1 = 0.f, s2 = 0.f, s1b = 0.f, s2b = 0.f;
#pragma unroll
      for (int i = 0; i < 6; i += 2) {
        s1 = dot2(hp[i], 0x3f803f80u, s1); s2 = dot2(hp[i], hp[i], s2);
        s1b = dot2(hp[i + 1], 0x3f803f80u, s1b); s2b = dot2(hp[i + 1], hp[i + 1], s2b);
      }
      float st = pair32(s1 + s1b, s2 + s2b);
      st = row_sum(pair16(st, st)) * (1.0f / D);
      const float mean = __uint_as_float(__builtin_amdgcn_readlane(__float_as_uint(st), 0)), msq = __uint_as_float(__builtin_amdgcn_readlane(__float_as_uint(st), 32));
      const unsigned mv = cvtpk_a(mean, fmx(fmaf(-mean, mean, msq), 0.f));
      const float nmb = -lo16(mv), den = rbf(__builtin_amdgcn_sqrtf(rbf(hi16(mv) + 1e-5f))), rden = __builtin_amdgcn_rcpf(den);
      unsigned e[6], o[6];
#pragma unroll
      for (int i = 0; i < 6; ++i) { const f32x2 t = f32x2{lo16(hp[i]), hi16(hp[i])} + nmb; e[i] = cvtpk_a(t.x, t.y); }
#pragma unroll
      for (int i = 0; i < 6; ++i) { const f32x2 t = f32x2{lo16(e[i]), hi16(e[i])} * rden; o[i] = cvtpk(t.x, t.y); }
      *(LAS u32x2*)(L + a_x0) = u32x2{o[0], o[1]}; *(LAS u32x2*)(L + a_x0 + 512) = u32x2{o[2], o[3]};
      *(LAS u32x2*)(L + a_x2) = u32x2{o[4], o[5]};
    }
    BAR();
    {
      u32x4 af[9];
#pragma unroll
      for (int i = 0; i < 9; ++i) af[i] = *(LAS u32x4*)(L + a_xn + i * 64);
      SCHED();
      f32x4 c = {0.f, 0.f, 0.f, 0.f}, c2 = c;
#pragma unroll
      for (int i = 0; i < 8; i += 2) { c = mfma(af[i], bg[i], c); c2 = mfma(af[i + 1], bg[i + 1], c2); }
      c = mfma(af[8], bg[8], c);
      *(LAS f32x2*)(L + a_gw) = f32x2{c[0] + c2[0], c[1] + c2[1]};
    }
    BAR();
    if (wave >= 2) {
      LAS float* gu = (LAS float*)(L + a_gr);
      const float g0 = gu[0], g1 = gu[64], u0 = gu[128], u1 = gu[192];
      SCHED();
      const unsigned gb = cvtpk_a(g0 + g1, u0 + u1);
      const float g = lo16(gb), u = hi16(gb);
      constexpr float K0 = -2.0f * LOG2E * 0.7978845608028654f;
      const float dn = 1.0f + __builtin_amdgcn_exp2f(g * fmaf(g * g, K0 * 0.044715f, K0));
      const float ge = rbf(g * __builtin_amdgcn_rcpf(dn));
      *(LAS u16*)(L + a_aw) = (u16)cvtpk(ge * u, 0.f);
      LGKM0();
      const u32x4 a = *(LAS u32x4*)(L + a_ar);
      const f32x4 z = {0.f, 0.f, 0.f, 0.f};
      f32x4 c[6];
#pragma unroll
      for (int i = 0; i < 6; ++i) c[i] = mfma(a, bd[i], z);
      publish_tiles<6>(rws, c, o4, o1, km, lane, tg);
    }
    return;
  }
  if (is_wo) {
    u32x4 bw[5][2];
#pragma unroll
    for (int ks = 0; ks < 2; ++ks) {
#pragma unroll
      for (int i = 0; i < 4; ++i) bw[i][ks] = bfrag576<false>(L, L_WO, wave + 8 * i, ks, lane);
      bw[4][ks] = bfrag576<true>(L, L_WOT, wave & 3, ks, lane);
    }
    const int yb = WS_YP + (b * 32 + hw) * 128 + (lane & 14) * 8;
    int o4 = (lane & 1) ? OOB : yb + (wave + 8 * (lane >> 4)) * YTS;
    int o1 = ((lane & 49) || wave >= 4) ? OOB : yb + (32 + wave) * YTS;
    int a_or = L_O16 + (lane & 1) * 128 + (lane >> 4) * 16;
    PIN(o4); PIN(o1); PIN(a_or);
    if (wave == 0) {
      int off[4]; u32x4 v[4];
      const int base = WS_AP + ((hw * 8 + (lane >> 5) * 4 + ((lane >> 4) & 1)) * 2 + b) * 384;
      off[0] = base + (lane & 15) * 16; off[1] = off[0] + 4 * 384; off[2] = base + 256; off[3] = base + 256 + 4 * 384;
      int a_ow = L_O16 + (lane >> 5) * 128 + (lane & 15) * 8 + ((lane >> 4) & 1) * 4;
      PIN(a_ow);
      gather<4, true>(rws, off, v, tg);
      const float m0 = __uint_as_float(v[2].x), m1 = __uint_as_float(v[3].x);
      const float M = x16_max(fmx(m0, m1));
      const float e0 = __builtin_amdgcn_exp2f((m0 - M) * LOG2E), e1 = __builtin_amdgcn_exp2f((m1 - M) * LOG2E);
      const float lp = fmaf(e0, __uint_as_float(v[2].y), e1 * __uint_as_float(v[3].y));
      const float rl = __builtin_amdgcn_rcpf(pair16(lp, lp));
      const f32x4 o = e0 * __builtin_bit_cast(f32x4, v[0]) + e1 * __builtin_bit_cast(f32x4, v[1]);
      *(LAS unsigned*)(L + a_ow) = cvtpk(pair16(o[0], o[2]) * rl, pair16(o[1], o[3]) * rl);
    }
    BAR();
    const u32x4 a0 = *(LAS u32x4*)(L + a_or), a1 = *(LAS u32x4*)(L + a_or + 64);
    const f32x4 z = {0.f, 0.f, 0.f, 0.f};
    f32x4 c[5];
#pragma unroll
    for (int i = 0; i < 4; ++i) c[i] = mfma(a0, bw[i][0], z);
#pragma unroll
    for (int i = 0; i < 4; ++i) c[i] = mfma(a1, bw[i][1], c[i]);
    SCHED();
    c[4] = mfma(a1, bw[4][1], mfma(a0, bw[4][0], z));
    publish_tiles<5>(rws, c, o4, o1, km, lane, tg);
  }
  if (rho >= 0 && wave == 0) {
    const int uo = (lane >> 2) * 128 + (rho & 1) * 64 + (lane & 3) * 16;
    float xf = lo16(xr), hf;
    int oh = (lane & 1) ? OOB : WS_HP + (((lane >> 2) & 3) + (lane >> 5) * 4) * HRS + rho * 128 + (lane & 16) * 2 + (lane & 2) * 8;
    int oo = (lane & 12) ? OOB : rcol * 2, oe = (rho == 0 && lane == 0) ? WS_EPOCH : OOB;
    PIN(xf); PIN(oh); PIN(oo); PIN(oe);
    {
      int off[4]; u32x4 v[4];
#pragma unroll
      for (int i = 0; i < 4; ++i) off[i] = WS_YP + (rho >> 1) * YTS + i * 2048 + uo;
      gather<4, true>(rws, off, v, tg);
      const unsigned hb = cvtpk(xf + rbf(reduce_units<4>(v)), 0.f);
      hf = lo16(hb);
      auto sw = __builtin_amdgcn_permlane32_swap(hb, hb, false, false);
      const unsigned pk = sw[0] | (sw[1] << 16);
      st_sc1(rws, oh, u32x4{pk, tg.a, dppu<XOR1>(pk), tg.b});
    }
    {
      int off[9]; u32x4 v[9];
#pragma unroll
      for (int i = 0; i < 9; ++i) off[i] = WS_OP + (rho >> 1) * OTS + i * 2048 + uo;
      const rsrc_t ro = mkrsrc(out, 2 * D * 2);
      {
        const int o_[1] = {off[4] - (lane >> 2) * 128}; u32x4 v_[1];
        gather<1, true>(rws, o_, v_, tg);
      }
      gather<9, true>(rws, off, v, tg);
      const unsigned r = cvtpk(hf + rbf(reduce_units<9>(v)), 0.f);
      __builtin_amdgcn_raw_buffer_store_b16((u16)r, ro, oo, 0, 0);
      __builtin_amdgcn_raw_buffer_store_b64(u32x2{tg.a, tg.b}, rws, oe, 0, 16);
    }
  }
}

extern "C" void kernel_launch(void* const* d_in, const int* in_sizes, int n_in, void* d_out, int out_size, void* d_ws, size_t ws_size, hipStream_t stream) {
  (void)hipFuncSetAttribute((const void*)fused_block, hipFuncAttributeMaxDynamicSharedMemorySize, L_END);
  unsigned char* ws = (unsigned char*)d_ws + ((ws_size - ((size_t)4 << 20)) & ~(size_t)0x1fffff);
  fused_block<<<NWG, NTHR, L_END, stream>>>((const u16*)d_in[0], (const u16*)d_in[1], (const u16*)d_in[2], (const u16*)d_in[3], (const u16*)d_in[4],
                                           (const u16*)d_in[6], (const u16*)d_in[7], (const u16*)d_in[8], (u16*)d_out, ws);
}
```
